```python
import torch

def _softmax(x, axis=-1):
    return torch.softmax(x, dim=axis)

def _gelu(x):
    return torch.nn.functional.gelu(x, approximate='tanh')

def _silu(x):
    return torch.nn.functional.silu(x)

def _sigmoid(x):
    return torch.sigmoid(x)

def _layernorm(x, axis=-1, eps=1e-05):
    m = x.mean(dim=axis, keepdim=True)
    v = x.var(dim=axis, keepdim=True, unbiased=False)
    return (x - m) / torch.sqrt(v + eps)

def _scatter_add(tgt, idx, src, axis=0):
    return tgt.clone().index_add_(axis, idx, src)

def _segment_sum(src, seg, n):
    out = torch.zeros((n,) + src.shape[1:], dtype=src.dtype)
    return out.index_add_(0, seg, src)

def _pad(x, pw):
    flat = tuple((v for pair in reversed(pw) for v in pair))
    return torch.nn.functional.pad(x, flat)
kernel_name = 'parallel_sum__ssm_conv_s4d__mixer_gmlp__prompt'

def setup_inputs():

    _w = lambda a: (lambda t: t.to(torch.bfloat16) if t.is_floating_point() else t)(torch.as_tensor(a))

    def _A_setup():
        Bsz, L, D, N = (1, 2048, 640, 128)
        return {'u': _w((lambda *s: torch.randn(*s, dtype=torch.bfloat16) * 0.05)(Bsz, L, D)), 'log_A_real': _w((lambda *s: torch.randn(*s, dtype=torch.bfloat16) * 3.2)(D, N) * 0.5), 'A_imag': _w((lambda *s: torch.randn(*s, dtype=torch.bfloat16) * 0.1)(D, N) * 3.14), 'C_real': _w((lambda *s: torch.randn(*s, dtype=torch.bfloat16) * 3.2)(D, N) / N ** 0.5), 'C_imag': _w((lambda *s: torch.randn(*s, dtype=torch.bfloat16) * 3.2)(D, N) / N ** 0.5), 'log_dt': _w((lambda *s: torch.randn(*s, dtype=torch.bfloat16) * 0.1)(D) * 0.5 - 4.0), 'D_skip': _w((lambda *s: torch.randn(*s, dtype=torch.bfloat16) * 0.4)(D))}
    _A_in = _A_setup()

    def _B_setup():
        B, S, D, F = (1, 2048, 640, 1728)
        Fh = F // 2
        return {'x': _w((lambda *s: torch.randn(*s, dtype=torch.bfloat16) * 0.05)(B, S, D)), 'w_in': _w((lambda *s: torch.randn(*s, dtype=torch.bfloat16) * 0.2)(D, 2 * Fh)), 'w_sp': _w(0.01 * (lambda *s: torch.randn(*s, dtype=torch.bfloat16) * 0.2)(S, S)), 'b_sp': _w(1.0 + 0.01 * (lambda *s: torch.randn(*s, dtype=torch.bfloat16) * 0.05)(S)), 'w_out': _w((lambda *s: torch.randn(*s, dtype=torch.bfloat16) * 0.05)(Fh, D))}
    _B_in = _B_setup()
    return {'A_u': _A_in['u'], 'A_log_A_real': _A_in['log_A_real'], 'A_A_imag': _A_in['A_imag'], 'A_C_real': _A_in['C_real'], 'A_C_imag': _A_in['C_imag'], 'A_log_dt': _A_in['log_dt'], 'A_D_skip': _A_in['D_skip'], 'B_x': _B_in['x'], 'B_w_in': _B_in['w_in'], 'B_w_sp': _B_in['w_sp'], 'B_b_sp': _B_in['b_sp'], 'B_w_out': _B_in['w_out']}

def reference(A_u, A_log_A_real, A_A_imag, A_C_real, A_C_imag, A_log_dt, A_D_skip, B_x, B_w_in, B_w_sp, B_b_sp, B_w_out):

    def _A_ref(u, log_A_real, A_imag, C_real, C_imag, log_dt, D_skip):
        L = u.shape[1]
        dt = torch.exp(log_dt)
        A = -torch.exp(log_A_real) + 1j * A_imag
        C = (C_real + 1j * C_imag) * dt[:, None]
        pos = (lambda x, dt: x.to(dt))(torch.arange(L), log_dt.dtype)
        P = torch.exp((A * dt[:, None])[:, :, None] * pos[None, None, :])
        K = 2.0 * torch.einsum('dn,dnl->dl', C, P).real
        L2 = 2 * L
        u_t = (lambda x, *d: x.permute(*d))(u, 0, 2, 1)
        Kf = (lambda x, *a, **k: torch.fft.rfft(x.float(), *a, **k))(K, L2)
        Uf = (lambda x, *a, **k: torch.fft.rfft(x.float(), *a, **k))(u_t, L2)
        y = (lambda x, *a, **k: torch.fft.irfft(x, *a, **k).to(torch.bfloat16))(Uf * Kf[None, :, :], L2)[:, :, :L]
        y = (lambda x, *d: x.permute(*d))(y, 0, 2, 1)
        return y + u * D_skip[None, None, :]

    def _B_ref(x, w_in, w_sp, b_sp, w_out):
        B, S, D = x.shape
        F = w_in.shape[-1]
        Fh = F // 2
        z = _gelu(torch.einsum('bsd,df->bsf', x, w_in))
        z1 = z[..., :Fh].clone()
        z2 = _layernorm(z[..., Fh:], axis=-1)
        g = torch.einsum('ts,bsf->btf', w_sp, z2) + b_sp[None, :, None]
        return torch.einsum('bsf,fd->bsd', z1 * g, w_out)
    x = A_u
    xn = _layernorm(x, axis=-1)
    return x + _A_ref(xn, A_log_A_real, A_A_imag, A_C_real, A_C_imag, A_log_dt, A_D_skip) + _B_ref(xn, B_w_in, B_w_sp, B_b_sp, B_w_out)
```

```cpp
#include <hip/hip_runtime.h>
#include <hip/hip_bf16.h>
#include <stdint.h>
#define POS_BF16 1
#define MIMIC 0
typedef unsigned short u16;
__device__ __forceinline__ float bf2f(u16 v){ return __uint_as_float(((unsigned)v)<<16); }
__device__ __forceinline__ u16 f2bf(float f){ unsigned u=__float_as_uint(f); return (u16)((u + 0x7FFF + ((u>>16)&1))>>16); }
__device__ __forceinline__ float rb(float f){
  return f;
}
constexpr int L=2048, D=640, N=128, F=1728, FH=864;

__global__ void ln_k(const u16* in, int ld, int c0, int n, u16* out){
  int row=blockIdx.x; int t=threadIdx.x;
  __shared__ float red[256];
  float s=0; for(int i=t;i<n;i+=256) s+=bf2f(in[row*ld+c0+i]);
  red[t]=s; __syncthreads();
  for(int o=128;o>0;o>>=1){ if(t<o) red[t]+=red[t+o]; __syncthreads(); }
  float mean=red[0]/n; __syncthreads();
  float q=0; for(int i=t;i<n;i+=256){ float d=bf2f(in[row*ld+c0+i])-mean; q+=d*d; }
  red[t]=q; __syncthreads();
  for(int o=128;o>0;o>>=1){ if(t<o) red[t]+=red[t+o]; __syncthreads(); }
  float var=red[0]/n;
  float m=rb(mean), v=rb(var);
  float den=rb(sqrtf(rb(v+1e-5f)));
  for(int i=t;i<n;i+=256){ float d=rb(bf2f(in[row*ld+c0+i])-m); out[row*n+i]=f2bf(d/den); }
}
__global__ void kgen_k(const u16* lar,const u16* aim,const u16* cr,const u16* ci,const u16* ldt,float* K){
  int d=blockIdx.x;
  float dt=rb(expf(bf2f(ldt[d])));
  for(int l=threadIdx.x;l<L;l+=blockDim.x){
    float p=bf2f(f2bf((float)l));
    float acc=0;
    for(int n=0;n<N;n++){
      float ar=-rb(expf(bf2f(lar[d*N+n])))*dt, ai=bf2f(aim[d*N+n])*dt;
      float Cr=bf2f(cr[d*N+n])*dt, Ci=bf2f(ci[d*N+n])*dt;
      float e=expf(ar*p); float sn,cs; sincosf(ai*p,&sn,&cs);
      acc+=Cr*e*cs-Ci*e*sn;
    }
    K[d*L+l]=2.f*acc;
  }
}
__global__ void conv_k(const u16* xn,const float* K,const u16* dskip,u16* ya){
  int d=blockIdx.x; __shared__ float su[L], sk[L];
  for(int l=threadIdx.x;l<L;l+=blockDim.x){ su[l]=bf2f(xn[l*D+d]); sk[l]=K[d*L+l]; }
  __syncthreads();
  for(int l=threadIdx.x;l<L;l+=blockDim.x){
    float a=0; for(int j=0;j<=l;j++) a+=sk[j]*su[l-j];
    float y=rb(a); float s=rb(su[l]*bf2f(dskip[d]));
    ya[l*D+d]=f2bf(y+s);
  }
}
__global__ void gemm_k(const u16* A,int lda,const u16* B,int ldb,int M,int Nn,int Kk,int mode,u16* out,
                       const u16* e0,int lde0,const u16* e1){
  __shared__ float sa[16][17], sb[16][17];
  int tx=threadIdx.x&15, ty=threadIdx.x>>4; int r=blockIdx.y*16+ty, c=blockIdx.x*16+tx;
  float acc=0;
  for(int k=0;k<Kk;k+=16){
    sa[ty][tx]=bf2f(A[r*lda+k+tx]); sb[ty][tx]=bf2f(B[(k+ty)*ldb+c]); __syncthreads();
    for(int j=0;j<16;j++) acc+=sa[ty][j]*sb[j][tx];
    __syncthreads();
  }
  if(mode==0){ float x=rb(acc); float u=0.7978845608028654f*(x+0.044715f*x*x*x); out[r*Nn+c]=f2bf(0.5f*x*(1.f+tanhf(u))); }
  else if(mode==1){ float g=rb(rb(acc)+bf2f(e1[r])); out[r*Nn+c]=f2bf(bf2f(e0[r*lde0+c])*g); }
  else { float b=rb(acc); float s=rb(bf2f(e0[r*lde0+c])+bf2f(e1[r*Nn+c])); out[r*Nn+c]=f2bf(s+b); }
}
extern "C" void kernel_launch(void* const* d_in, const int* in_sizes, int n_in, void* d_out, int out_size, void* d_ws, size_t ws_size, hipStream_t stream){
  const u16* x=(const u16*)d_in[0]; const u16* lar=(const u16*)d_in[1]; const u16* aim=(const u16*)d_in[2];
  const u16* cr=(const u16*)d_in[3]; const u16* ci=(const u16*)d_in[4]; const u16* ldt=(const u16*)d_in[5]; const u16* dsk=(const u16*)d_in[6];
  const u16* win=(const u16*)d_in[8]; const u16* wsp=(const u16*)d_in[9]; const u16* bsp=(const u16*)d_in[10]; const u16* wout=(const u16*)d_in[11];
  char* ws=(char*)d_ws;
  u16* xn=(u16*)ws; ws+=L*D*2;
  float* K=(float*)ws; ws+=D*L*4;
  u16* ya=(u16*)ws; ws+=L*D*2;
  u16* z=(u16*)ws; ws+=L*F*2;
  u16* z2n=(u16*)ws; ws+=L*FH*2;
  u16* h=(u16*)ws; ws+=L*FH*2;
  ln_k<<<L,256,0,stream>>>(x,D,0,D,xn);
  kgen_k<<<D,256,0,stream>>>(lar,aim,cr,ci,ldt,K);
  conv_k<<<D,256,0,stream>>>(xn,K,dsk,ya);
  gemm_k<<<dim3(F/16,L/16),256,0,stream>>>(xn,D,win,F,L,F,D,0,z,nullptr,0,nullptr);
  ln_k<<<L,256,0,stream>>>(z,F,FH,FH,z2n);
  gemm_k<<<dim3(FH/16,L/16),256,0,stream>>>(wsp,L,z2n,FH,L,FH,L,1,h,z,F,bsp);
  gemm_k<<<dim3(D/16,L/16),256,0,stream>>>(h,FH,wout,D,L,D,FH,2,(u16*)d_out,x,D,ya);
}
```

```cpp
#include <hip/hip_runtime.h>
#include <stdint.h>

typedef unsigned short u16;
typedef unsigned int u32;
typedef __attribute__((ext_vector_type(8))) __bf16 bf16x8;
typedef __attribute__((ext_vector_type(2))) __bf16 bf16x2;
typedef __attribute__((ext_vector_type(8))) _Float16 f16x8;
typedef __attribute__((ext_vector_type(2))) _Float16 f16x2;
typedef __attribute__((ext_vector_type(4))) short s16x4;
typedef __attribute__((ext_vector_type(4))) float f32x4;
typedef __attribute__((ext_vector_type(2))) float f32x2;
typedef __attribute__((ext_vector_type(4))) u32 u32x4;
typedef __attribute__((ext_vector_type(2))) u32 u32x2;
#define LAS __attribute__((address_space(3)))
#define GAS __attribute__((address_space(1)))
#define DEVI __device__ __forceinline__

constexpr int L = 2048, D = 640, F = 1728, FH = 864, NM = 128;

constexpr size_t OFF_Z2F = 0;
constexpr size_t OFF_Z1A = OFF_Z2F + 54 * 64 * 1024;
constexpr size_t OFF_ST = OFF_Z1A + 128 * 54 * 512;
constexpr size_t OFF_UT = OFF_ST + 2048 * 32;
constexpr size_t OFF_KR = OFF_UT + 640 * 4096;
constexpr size_t OFF_WOF = OFF_KR + 640 * 8192;
constexpr size_t OFF_HF = OFF_WOF + 40 * 27 * 1024;
constexpr size_t OFF_YT = OFF_HF + 128 * 27 * 1024;

DEVI float bflo(u32 v) { return __uint_as_float(v << 16); }
DEVI float bfhi(u32 v) { return __uint_as_float(v & 0xffff0000u); }
DEVI float bf1(u16 v) { return __uint_as_float((u32)v << 16); }
DEVI u32 pkbf(float a, float b) {
  f32x2 x = {a, b};
  return __builtin_bit_cast(u32, __builtin_convertvector(x, bf16x2));
}
DEVI u32 pkf16(float a, float b) {
  f32x2 x = {a, b};
  return __builtin_bit_cast(u32, __builtin_convertvector(x, f16x2));
}
DEVI float rbf(float a) { return bflo(pkbf(a, a)); }
DEVI f32x4 mma(u32x4 a, u32x4 b, f32x4 c) {
  return __builtin_amdgcn_mfma_f32_16x16x32_bf16(__builtin_bit_cast(bf16x8, a), __builtin_bit_cast(bf16x8, b), c, 0, 0, 0);
}
DEVI f32x4 mmah(u32x4 a, u32x4 b, f32x4 c) {
  return __builtin_amdgcn_mfma_f32_16x16x32_f16(__builtin_bit_cast(f16x8, a), __builtin_bit_cast(f16x8, b), c, 0, 0, 0);
}
DEVI void glds16(const void* g, LAS char* l) {
  __builtin_amdgcn_global_load_lds((const GAS u32*)g, (LAS u32*)l, 16, 0, 0);
}
DEVI void glds4(const void* g, LAS char* l) {
  __builtin_amdgcn_global_load_lds((const GAS u32*)g, (LAS u32*)l, 4, 0, 0);
}
DEVI u32x2 trread(LAS char* p) {
  return __builtin_bit_cast(u32x2, __builtin_amdgcn_ds_read_tr16_b64_v4i16((LAS s16x4*)p));
}
DEVI u32x4 ld128(LAS char* p) { return *(LAS u32x4*)p; }
DEVI u32x2 ld64(LAS char* p) { return *(LAS u32x2*)p; }
template <int CTRL> DEVI float dppf(float v) {
  return __int_as_float(__builtin_amdgcn_update_dpp(0, __float_as_int(v), CTRL, 0xf, 0xf, true));
}
template <int CTRL> DEVI u32 dppu(u32 v) { return (u32)__builtin_amdgcn_update_dpp(0, (int)v, CTRL, 0xf, 0xf, true); }
DEVI float sum8(float v) {
  v += dppf<0xB1>(v);
  v += dppf<0x4E>(v);
  v += dppf<0x141>(v);
  return v;
}
DEVI float sum16(float v) {
  v = sum8(v);
  v += dppf<0x140>(v);
  return v;
}
#define WAITV(n) asm volatile("s_waitcnt vmcnt(%0)" ::"n"(n) : "memory")
#define WAITL() asm volatile("s_waitcnt lgkmcnt(0)" ::: "memory")
#define BAR() __builtin_amdgcn_s_barrier()
#define FENCE() asm volatile("" ::: "memory")

DEVI float gelu(float x) {
  const float c1 = -2.0f * 0.7978845608028654f * 1.4426950408889634f;
  const float c2 = c1 * 0.044715f;
  float t = x * __builtin_fmaf(c2, x * x, c1);
  float e = __builtin_amdgcn_exp2f(t);
  return x * __builtin_amdgcn_rcpf(1.0f + e);
}

constexpr int K1_A = 0;
constexpr int K1_RING = 81920;
constexpr int K1_MISC = 147456;
constexpr int K1_LDS = 147456 + 4096;

template <int MODE>
DEVI void k1_loop(LAS char* sm, const char* wsrc, int w, int lane, f32x4 (&acc)[2][4], char* ut, int t0) {
  const int unit = w & 3, h = w >> 2;
  const int r = lane & 15, q = lane >> 4;
  const int a_off0 = ((4 * h + (r >> 3)) * 10) * 1024 + (r & 7) * 128 + ((q ^ (r & 7)) * 16);
  const int a_off1 = a_off0 ^ 64;
  const int brow = 8 * q + (r >> 2);
  const int bs = ((brow >> 1) & 1) | (((brow >> 3) & 1) << 1);
  const int b_off = unit * 4096 + brow * 128 + (r & 3) * 8;
  LAS char* dst = sm + K1_RING + unit * 4096 + h * 1024;
#pragma unroll 1
  for (int s = 0; s < 20; ++s) {
    if (MODE != 3) {
      if (s < 18) WAITV(4);
      else if (s == 18) WAITV(2);
      else WAITV(0);
    }
    BAR();
    FENCE();
    if (MODE != 3) {
      if (s + 3 < 20) {
        const char* g = wsrc + (size_t)(s + 3) * 32 * (F * 2);
        LAS char* d = dst + ((s + 3) & 3) * 16384;
        glds16(g, d);
        glds16(g + 16 * (F * 2), d + 2048);
      }
      const int ao = ((s & 1) ? a_off1 : a_off0) + (s >> 1) * 1024;
      u32x4 a0 = ld128(sm + K1_A + ao);
      u32x4 a1 = ld128(sm + K1_A + ao + 20 * 1024);
      LAS char* bp = sm + K1_RING + (s & 3) * 16384 + b_off;
      u32x4 bf[4];
#pragma unroll
      for (int f = 0; f < 4; ++f) {
        u32x2 lo = trread(bp + ((f ^ bs) * 32));
        u32x2 hi = trread(bp + ((f ^ bs) * 32) + 512);
        bf[f] = u32x4{lo[0], lo[1], hi[0], hi[1]};
      }
#pragma unroll
      for (int f = 0; f < 4; ++f) {
        const bool sw = (MODE == 0) || (MODE == 2 && f < 2);
        if (sw) {
          acc[0][f] = mma(bf[f], a0, acc[0][f]);
          acc[1][f] = mma(bf[f], a1, acc[1][f]);
        } else {
          acc[0][f] = mma(a0, bf[f], acc[0][f]);
          acc[1][f] = mma(a1, bf[f], acc[1][f]);
        }
      }
    } else {
      const int ch = 4 * (s & 1) + 2 * h + ((r & 3) >> 1);
      const int r0 = r >> 2;
#pragma unroll
      for (int i = 0; i < 2; ++i) {
        const int tg = 4 * i + q;
        LAS char* base = sm + K1_A + (tg * 10 + (s >> 1)) * 1024 + (r & 1) * 8;
        u32x2 lo = trread(base + r0 * 128 + ((ch ^ r0) * 16));
        u32x2 hi = trread(base + (r0 + 4) * 128 + ((ch ^ (r0 + 4)) * 16));
        u32x4 v = {lo[0], lo[1], hi[0], hi[1]};
        *(GAS u32x4*)(ut + (size_t)(32 * s + 16 * h + r) * 4096 + (t0 + tg * 8) * 2) = v;
      }
    }
  }
}

template <int MODE>
DEVI void k1_epi(f32x4 (&acc)[2][4], char* ws, int rp, int gu, int h, int lane, float (&s1)[8], float (&s2)[8]) {
#pragma unroll
  for (int f = 0; f < 4; ++f) {
    const bool sw = (MODE == 0) || (MODE == 2 && f < 2);
    const int nf = gu * 4 + f;
    u32 o[4];
#pragma unroll
    for (int t = 0; t < 2; ++t)
#pragma unroll
      for (int i = 0; i < 2; ++i) {
        u32 pre = pkbf(acc[t][f][2 * i], acc[t][f][2 * i + 1]);
        o[2 * t + i] = pkbf(gelu(bflo(pre)), gelu(bfhi(pre)));
      }
    if (sw) {
#pragma unroll
      for (int t = 0; t < 2; ++t)
        *(GAS u32x2*)(ws + OFF_Z1A + (size_t)((rp * 4 + 2 * h + t) * 54 + nf) * 512 + lane * 8) = u32x2{o[2 * t], o[2 * t + 1]};
    } else {
      *(GAS u32x4*)(ws + OFF_Z2F + (size_t)((nf - 54) * 64 + rp * 2 + h) * 1024 + lane * 16) = u32x4{o[0], o[1], o[2], o[3]};
#pragma unroll
      for (int i = 0; i < 4; ++i) {
        float a = bflo(o[i]), b = bfhi(o[i]);
        s1[2 * i] += a;
        s1[2 * i + 1] += b;
        s2[2 * i] = __builtin_fmaf(a, a, s2[2 * i]);
        s2[2 * i + 1] = __builtin_fmaf(b, b, s2[2 * i + 1]);
      }
    }
  }
}

DEVI void k1_gemm(LAS char* sm, int rp, int ct, int idx, int bid, const u16* x, const u16* win, const u16* wsp, char* ws) {
  const int tid = threadIdx.x, lane = tid & 63;
  const int w = __builtin_amdgcn_readfirstlane(tid >> 6);
  const int unit = w & 3, h = w >> 2;
  const int gu = ct * 4 + unit;
  const bool active = gu < 27;
  const int t0 = rp * 64;
  const int prow = lane >> 3, pp = lane & 7;
  {
    const char* g = (const char*)x + (size_t)(t0 + 8 * w + prow) * (D * 2) + ((pp ^ prow) * 16);
#pragma unroll
    for (int j = 0; j < 10; ++j) glds16(g + j * 128, sm + K1_A + (w * 10 + j) * 1024);
  }
  const int sz = ((prow >> 1) & 1) | (h << 1);
  const char* wsrc = (const char*)win + (size_t)(8 * h + prow) * (F * 2) + gu * 128 + (((pp >> 1) ^ sz) * 2 + (pp & 1)) * 16;
  if (active) {
    LAS char* dst = sm + K1_RING + unit * 4096 + h * 1024;
#pragma unroll
    for (int s = 0; s < 3; ++s) {
      glds16(wsrc + (size_t)s * 32 * (F * 2), dst + s * 16384);
      glds16(wsrc + (size_t)(s * 32 + 16) * (F * 2), dst + s * 16384 + 2048);
    }
  }
  LAS char* scratch = sm + K1_MISC + 2048 + w * 256;
  {
    int ln = idx * 640 + w * 80 + lane;
    glds4((const char*)win + (size_t)min(ln, 17279) * 128, scratch);
    glds4((const char*)win + (size_t)min(ln + ((lane < 16) ? 64 : 0), 17279) * 128, scratch);
  }
  if (active) WAITV(8);
  else WAITV(2);
  {
    float v[80];
    float s = 0.f;
#pragma unroll
    for (int j = 0; j < 10; ++j) {
      u32x4 c = ld128(sm + K1_A + (w * 10 + j) * 1024 + lane * 16);
#pragma unroll
      for (int i = 0; i < 4; ++i) {
        v[8 * j + 2 * i] = bflo(c[i]);
        v[8 * j + 2 * i + 1] = bfhi(c[i]);
      }
    }
    float sa = 0.f, sb = 0.f, sc = 0.f, sd = 0.f;
#pragma unroll
    for (int i = 0; i < 80; i += 4) { sa += v[i]; sb += v[i + 1]; sc += v[i + 2]; sd += v[i + 3]; }
    s = sum8((sa + sb) + (sc + sd));
    const float mean = s * (1.0f / D);
    const float mb = rbf(mean);
    sa = sb = sc = sd = 0.f;
#pragma unroll
    for (int i = 0; i < 80; i += 4) {
      v[i] -= mb; v[i + 1] -= mb; v[i + 2] -= mb; v[i + 3] -= mb;
      sa = __builtin_fmaf(v[i], v[i], sa);
      sb = __builtin_fmaf(v[i + 1], v[i + 1], sb);
      sc = __builtin_fmaf(v[i + 2], v[i + 2], sc);
      sd = __builtin_fmaf(v[i + 3], v[i + 3], sd);
    }
    const float dm = mean - mb;
    float var = sum8((sa + sb) + (sc + sd)) * (1.0f / D) - dm * dm;
    var = fmaxf(var, 0.f);
    const float den = rbf(__builtin_sqrtf(rbf(rbf(var) + 1e-5f)));
    const float ri = 1.0f / den;
#pragma unroll
    for (int j = 0; j < 10; ++j) {
      u32x4 c;
#pragma unroll
      for (int i = 0; i < 4; ++i) c[i] = pkbf(v[8 * j + 2 * i] * ri, v[8 * j + 2 * i + 1] * ri);
      *(LAS u32x4*)(sm + K1_A + (w * 10 + j) * 1024 + lane * 16) = c;
    }
  }
  WAITL();
  f32x4 acc[2][4];
#pragma unroll
  for (int t = 0; t < 2; ++t)
#pragma unroll
    for (int f = 0; f < 4; ++f) acc[t][f] = f32x4{0.f, 0.f, 0.f, 0.f};
  float s1[8], s2[8];
#pragma unroll
  for (int i = 0; i < 8; ++i) s1[i] = s2[i] = 0.f;
  const char* tsp = (const char*)wsp + (size_t)(bid * 256 + w * 32 + (lane & 31)) * 128;
  if (gu < 13) {
    k1_loop<0>(sm, wsrc, w, lane, acc, ws + OFF_UT, t0);
    glds4(tsp, scratch);
    k1_epi<0>(acc, ws, rp, gu, h, lane, s1, s2);
  } else if (gu == 13) {
    k1_loop<2>(sm, wsrc, w, lane, acc, ws + OFF_UT, t0);
    glds4(tsp, scratch);
    k1_epi<2>(acc, ws, rp, gu, h, lane, s1, s2);
  } else if (gu < 27) {
    k1_loop<1>(sm, wsrc, w, lane, acc, ws + OFF_UT, t0);
    glds4(tsp, scratch);
    k1_epi<1>(acc, ws, rp, gu, h, lane, s1, s2);
  } else {
    k1_loop<3>(sm, wsrc, w, lane, acc, ws + OFF_UT, t0);
    glds4(tsp, scratch);
  }
  if (ct >= 3) {
#pragma unroll
    for (int i = 0; i < 8; ++i) { s1[i] = sum16(s1[i]); s2[i] = sum16(s2[i]); }
    if ((lane & 15) == 0) {
      const int q = lane >> 4;
#pragma unroll
      for (int i = 0; i < 8; ++i) {
        const int row = 32 * h + 16 * (i >> 2) + 4 * q + (i & 3);
        *(LAS f32x2*)(sm + K1_MISC + (unit * 64 + row) * 8) = f32x2{s1[i], s2[i]};
      }
    }
    WAITL();
    BAR();
    FENCE();
    if (tid < 64) {
      f32x2 a = *(LAS f32x2*)(sm + K1_MISC + tid * 8);
      f32x2 b = *(LAS f32x2*)(sm + K1_MISC + (64 + tid) * 8);
      f32x2 c = *(LAS f32x2*)(sm + K1_MISC + (128 + tid) * 8);
      f32x2 d = *(LAS f32x2*)(sm + K1_MISC + (192 + tid) * 8);
      *(GAS f32x2*)(ws + OFF_ST + (size_t)(t0 + tid) * 32 + (ct - 3) * 8) = (a + b) + (c + d);
    }
  }
  WAITV(0);
}

struct cpx { float r, i; };
DEVI cpx cmul(cpx a, cpx b) {
  return cpx{__builtin_fmaf(a.r, b.r, -a.i * b.i), __builtin_fmaf(a.r, b.i, a.i * b.r)};
}
DEVI cpx cexp_p(float lr2, float wv, float p) {
  float e = __builtin_amdgcn_exp2f(lr2 * p);
  float ph = __builtin_amdgcn_fractf(wv * p);
  return cpx{e * __builtin_amdgcn_cosf(ph), e * __builtin_amdgcn_sinf(ph)};
}

DEVI void k1_aux(LAS char* sm0, int aid, int bid, const u16* lar, const u16* aim, const u16* cre, const u16* cim,
                 const u16* ldt, const u16* wout, const u16* wsp, char* ws) {
  const int tid = threadIdx.x, lane = tid & 63;
  const int w = __builtin_amdgcn_readfirstlane(tid >> 6);
  LAS char* sm = sm0 + w * 18432;
  const int r = lane & 15, q = lane >> 4;
  for (int u = aid * 8 + w; u < 270; u += 256) {
    const int fc = u / 10, cg = u - fc * 10;
    const int prow = lane >> 3, pp = lane & 7;
#pragma unroll
    for (int i = 0; i < 4; ++i)
      glds16((const char*)wout + (size_t)(fc * 32 + i * 8 + prow) * (D * 2) + cg * 128 + pp * 16, sm + i * 1024);
    WAITV(0);
#pragma unroll
    for (int f = 0; f < 4; ++f) {
      LAS char* p = sm + (4 * q + (r >> 2)) * 128 + f * 32 + (r & 3) * 8;
      u32x2 lo = trread(p), hi = trread(p + 2048);
      *(GAS u32x4*)(ws + OFF_WOF + (size_t)((cg * 4 + f) * 27 + fc) * 1024 + lane * 16) = u32x4{lo[0], lo[1], hi[0], hi[1]};
    }
    WAITL();
  }
  glds4((const char*)wsp + (size_t)(bid * 256 + w * 32 + (lane & 31)) * 128, sm + 16384);
  int goff[4][9];
#pragma unroll
  for (int i = 0; i < 4; ++i)
#pragma unroll
    for (int e = 0; e < 9; ++e) {
      int j = 2047 - 8 * (lane + 64 * i) - e;
      goff[i][e] = 2 * (int)rbf((float)max(j, 0));
    }
  const int wr = lane * 4;
#pragma unroll 1
  for (int c = w; c < 20; c += 8) {
    const int d = aid * 20 + c;
    const float dt = rbf(__builtin_amdgcn_exp2f(bf1(ldt[d]) * 1.4426950408889634f));
    float cr[2], ci[2], la[2], ai[2];
#pragma unroll
    for (int hh = 0; hh < 2; ++hh) {
      const int o = d * NM + hh * 64 + lane;
      cr[hh] = bf1(cre[o]); ci[hh] = bf1(cim[o]); la[hh] = bf1(lar[o]); ai[hh] = bf1(aim[o]);
    }
    float cs = fmaxf(fmaxf(fabsf(cr[0]), fabsf(ci[0])), fmaxf(fabsf(cr[1]), fabsf(ci[1])));
#pragma unroll
    for (int o = 32; o > 0; o >>= 1) cs = fmaxf(cs, __shfl_xor(cs, o));
    const float cinv = cs > 0.f ? 16384.0f / cs : 0.f;
    f32x4 acc[3] = {f32x4{0, 0, 0, 0}, f32x4{0, 0, 0, 0}, f32x4{0, 0, 0, 0}};
#pragma unroll 1
    for (int hh = 0; hh < 2; ++hh) {
      const float lav = hh ? la[1] : la[0], aiv = hh ? ai[1] : ai[0];
      const float crv = hh ? cr[1] : cr[0], civ = hh ? ci[1] : ci[0];
      const float lr2 = -rbf(__builtin_amdgcn_exp2f(lav * 1.4426950408889634f)) * dt * 1.4426950408889634f;
      const float wv = aiv * dt * 0.15915494309189535f;
      const cpx z1 = cexp_p(lr2, wv, 1.f), Z = cexp_p(lr2, wv, 128.f);
      cpx w8 = cexp_p(lr2, wv, 8.f);
      w8.i = -w8.i;
#define PUT(row, v) *(LAS u32*)(sm + (row) * 256 + (wr ^ (((row) & 15) << 4))) = pkf16((v).r, (v).i)
      cpx a[8];
      a[0] = cpx{crv * cinv, civ * cinv};
      PUT(0, a[0]);
#pragma unroll
      for (int i = 1; i < 8; ++i) { a[i] = cmul(a[i - 1], z1); PUT(i, a[i]); }
#pragma unroll
      for (int i = 0; i < 8; ++i) { a[i] = cmul(a[i], Z); PUT(8 + i, a[i]); }
#pragma unroll
      for (int k = 0; k < 2; ++k)
#pragma unroll
        for (int i = 0; i < 4; ++i) { a[2 * i] = cmul(a[2 * i], Z); PUT(16 + 4 * k + i, a[2 * i]); }
#pragma unroll
      for (int k = 0; k < 4; ++k)
#pragma unroll
        for (int i = 0; i < 2; ++i) { a[4 * i] = cmul(a[4 * i], Z); PUT(24 + 2 * k + i, a[4 * i]); }
#pragma unroll
      for (int k = 0; k < 9; ++k) { a[0] = cmul(a[0], Z); PUT(32 + k, a[0]); }
      cpx b = cpx{16384.0f, 0.f};
      PUT(48, b);
#pragma unroll
      for (int j = 1; j < 16; ++j) { b = cmul(b, w8); PUT(48 + j, b); }
#undef PUT
#pragma unroll
      for (int s = 0; s < 4; ++s) {
        LAS char* p = sm + r * 256 + (((4 * s + q) ^ r) * 16);
        u32x4 bfr = ld128(p + 48 * 256);
#pragma unroll
        for (int T = 0; T < 3; ++T) acc[T] = mmah(ld128(p + T * 4096), bfr, acc[T]);
      }
    }
    const float ks = 2.0f * dt * cs * (1.0f / (16384.0f * 16384.0f));
#pragma unroll
    for (int i = 0; i < 4; ++i) {
      const int row = 4 * q + i;
      const int p0 = (row & 7) + (row >> 3) * 128;
      const int p1 = row < 8 ? 256 + (row >> 2) * 128 + 2 * (row & 3) : 512 + ((row - 8) >> 1) * 128 + 4 * (row & 1);
      const int p2 = 1024 + row * 128;
      *(LAS u16*)(sm + (p0 + 8 * r) * 2) = (u16)pkbf(acc[0][i] * ks, 0.f);
      *(LAS u16*)(sm + (p1 + 8 * r) * 2) = (u16)pkbf(acc[1][i] * ks, 0.f);
      if (row <= 8) *(LAS u16*)(sm + (p2 + 8 * r) * 2) = (u16)pkbf(acc[2][i] * ks, 0.f);
    }
#pragma unroll
    for (int i = 0; i < 4; ++i) {
      u32 v[9];
#pragma unroll
      for (int e = 0; e < 9; ++e) v[e] = *(LAS u16*)(sm + goff[i][e]);
      if (i == 3 && lane == 63) v[8] = 0;
      u32x4 r1 = {v[0] | (v[1] << 16), v[2] | (v[3] << 16), v[4] | (v[5] << 16), v[6] | (v[7] << 16)};
      u32x4 r0 = {v[1] | (v[2] << 16), v[3] | (v[4] << 16), v[5] | (v[6] << 16), v[7] | (v[8] << 16)};
      char* o = ws + OFF_KR + (size_t)d * 8192 + (lane + 64 * i) * 16;
      *(GAS u32x4*)o = r0;
      *(GAS u32x4*)(o + 4096) = r1;
    }
  }
  WAITV(0);
}

__global__ __launch_bounds__(512, 2) void k1(const u16* x, const u16* lar, const u16* aim, const u16* cre, const u16* cim,
                                             const u16* ldt, const u16* win, const u16* wsp, const u16* wout, char* ws) {
  extern __shared__ __attribute__((aligned(16))) char smem[];
  LAS char* sm = (LAS char*)smem;
  const int b = blockIdx.x, xcd = b & 7, idx = b >> 3;
  if (idx < 28) k1_gemm(sm, xcd * 4 + (idx & 3), idx >> 2, idx, b, x, win, wsp, ws);
  else k1_aux(sm, xcd * 4 + (idx - 28), b, lar, aim, cre, cim, ldt, wout, wsp, ws);
}

constexpr int K2_NS = 8;
constexpr int K2_B = 0;
constexpr int K2_W = K2_NS * 12288;
constexpr int K2_R = K2_W + 24576;
constexpr int K2_M = K2_R + 8192;
constexpr int K2_C = K2_M + 4096;
constexpr int CV_SLOT = 17664, CV_R1 = 4224, CV_U2 = 8832, CV_U0 = 13472;
constexpr int K2_LDS = 8 * CV_SLOT;
static_assert(K2_C + 1024 <= K2_LDS, "lds");

DEVI void k2_gemm(LAS char* sm, int rt, int ct, const u16* wsp, const u16* bsp, char* ws) {
  const int tid = threadIdx.x, lane = tid & 63;
  const int w = __builtin_amdgcn_readfirstlane(tid >> 6);
  const int r = lane & 15, q = lane >> 4;
  if (w < 4) {
    const int tg = w >> 1, fg = w & 1;
    const char* zsrc[3];
#pragma unroll
    for (int i = 0; i < 3; ++i) {
      const int id = w * 3 + i;
      zsrc[i] = ws + OFF_Z2F + (size_t)((ct * 6 + (id >> 1)) * 64 + (id & 1)) * 1024 + lane * 16;
    }
    LAS char* dst = sm + K2_B + w * 3072;
#pragma unroll
    for (int s = 0; s < K2_NS - 1; ++s)
#pragma unroll
      for (int i = 0; i < 3; ++i) glds16(zsrc[i] + s * 2048, dst + s * 12288 + i * 1024);
    f32x4 acc[3][3], accc[3];
#pragma unroll
    for (int a = 0; a < 3; ++a) {
      accc[a] = f32x4{0, 0, 0, 0};
#pragma unroll
      for (int c = 0; c < 3; ++c) acc[a][c] = f32x4{0, 0, 0, 0};
    }
    const int wo0 = (tg * 6) * 1024 + ((r ^ (2 * (q >> 1))) + 16 * q) * 16;
    const int wo1 = (tg * 6 + 1) * 1024 + ((r ^ (2 * (q >> 1) + 12)) + 16 * q) * 16;
    const int zo = fg * 6 * 1024 + lane * 16;
    int slot = 0;
    BAR();
#pragma unroll 1
    for (int k = 0; k < 32; ++k) {
      if (k <= 32 - K2_NS + 1) WAITV(3 * (K2_NS - 2));
      else WAITV(0);
      BAR();
      FENCE();
      if (k + K2_NS - 1 < 32) {
        const int ps = slot == 0 ? K2_NS - 1 : slot - 1;
#pragma unroll
        for (int i = 0; i < 3; ++i) glds16(zsrc[i] + (k + K2_NS - 1) * 2048, dst + ps * 12288 + i * 1024);
      }
      LAS char* zb = sm + K2_B + slot * 12288 + zo;
      LAS char* wb = sm + K2_W + (k & 1) * 12288;
      u32x4 mf = ld128(sm + K2_M + (2 * k + fg) * 64 + q * 16);
#pragma unroll
      for (int kc = 0; kc < 2; ++kc) {
        u32x4 zf[3], wf[3];
#pragma unroll
        for (int i = 0; i < 3; ++i) {
          zf[i] = ld128(zb + (2 * i + kc) * 1024);
          wf[i] = ld128(wb + (kc ? wo1 : wo0) + i * 2048);
        }
#pragma unroll
        for (int a = 0; a < 3; ++a)
#pragma unroll
          for (int c = 0; c < 3; ++c) acc[a][c] = mma(zf[c], wf[a], acc[a][c]);
        if (kc == fg) {
#pragma unroll
          for (int a = 0; a < 3; ++a) accc[a] = mma(mf, wf[a], accc[a]);
        }
      }
      slot = slot == K2_NS - 1 ? 0 : slot + 1;
    }
    if (q == 0) {
#pragma unroll
      for (int a = 0; a < 3; ++a) *(LAS float*)(sm + K2_C + (fg * 96 + (tg * 3 + a) * 16 + r) * 4) = accc[a][0];
    }
    u32x2 z1v[3][3];
    float bb[3];
#pragma unroll
    for (int a = 0; a < 3; ++a) {
      const int tf = min(rt * 6 + tg * 3 + a, 127);
      bb[a] = bf1(bsp[tf * 16 + r]);
#pragma unroll
      for (int c = 0; c < 3; ++c)
        z1v[a][c] = *(const GAS u32x2*)(ws + OFF_Z1A + (size_t)(tf * 54 + ct * 6 + fg * 3 + c) * 512 + lane * 8);
    }
    WAITL();
    BAR();
    FENCE();
#pragma unroll
    for (int a = 0; a < 3; ++a) {
      const int tf = rt * 6 + tg * 3 + a;
      const float cc = *(LAS float*)(sm + K2_C + ((tg * 3 + a) * 16 + r) * 4) + *(LAS float*)(sm + K2_C + (96 + (tg * 3 + a) * 16 + r) * 4);
      if (tf < 128) {
#pragma unroll
        for (int c = 0; c < 3; ++c) {
          u32x2 o;
#pragma unroll
          for (int i = 0; i < 2; ++i) {
            u32 g = pkbf(acc[a][c][2 * i] - cc, acc[a][c][2 * i + 1] - cc);
            u32 gb = pkbf(bflo(g) + bb[a], bfhi(g) + bb[a]);
            o[i] = pkbf(bflo(z1v[a][c][i]) * bflo(gb), bfhi(z1v[a][c][i]) * bfhi(gb));
          }
          const int ff = ct * 6 + fg * 3 + c;
          *(GAS u32x2*)(ws + OFF_HF + (size_t)(tf * 27 + (ff >> 1)) * 1024 + lane * 16 + (ff & 1) * 8) = o;
        }
      }
    }
  } else {
    const int pt = tid - 256;
    f32x4 st[8][2];
#pragma unroll
    for (int i = 0; i < 8; ++i) {
      const char* p = ws + OFF_ST + (size_t)(i * 256 + pt) * 32;
      st[i][0] = *(const GAS f32x4*)p;
      st[i][1] = *(const GAS f32x4*)(p + 16);
    }
    const int c = pt & 7;
    const char* wsrc[3];
    int wdst[3];
#pragma unroll
    for (int i = 0; i < 3; ++i) {
      const int row = i * 32 + (pt >> 3);
      wsrc[i] = (const char*)wsp + (size_t)min(rt * 96 + row, 2047) * 4096 + c * 16;
      const int kc = c >> 2, q0 = (2 * c) & 3, hf = (c >> 1) & 1;
      wdst[i] = ((row >> 4) * 2 + kc) * 1024 + (((row & 15) ^ (q0 + 12 * kc)) + 16 * q0) * 16 + hf * 8;
    }
    constexpr int PD = 4;
    u32x4 wv[PD][3];
#pragma unroll
    for (int s = 0; s < PD; ++s)
#pragma unroll
      for (int i = 0; i < 3; ++i) wv[s][i] = *(const GAS u32x4*)(wsrc[i] + s * 128);
#pragma unroll
    for (int i = 0; i < 8; ++i) {
      const int s = i * 256 + pt;
      const float s1 = (st[i][0][0] + st[i][0][2]) + (st[i][1][0] + st[i][1][2]);
      const float s2 = (st[i][0][1] + st[i][0][3]) + (st[i][1][1] + st[i][1][3]);
      const float mean = s1 * (1.0f / FH);
      const float var = fmaxf(s2 * (1.0f / FH) - mean * mean, 0.f);
      const float den = rbf(__builtin_sqrtf(rbf(rbf(var) + 1e-5f)));
      *(LAS float*)(sm + K2_R + s * 4) = 1.0f / den;
      *(LAS u16*)(sm + K2_M + ((s >> 5) * 32 + ((s & 15) >> 2) * 8 + ((s >> 4) & 1) * 4 + (s & 3)) * 2) = (u16)pkbf(mean, 0.f);
    }
    WAITL();
    BAR();
    FENCE();
    auto put = [&](u32x4 (&v)[3], int k) {
      f32x4 ra = *(LAS f32x4*)(sm + K2_R + (k * 64 + c * 8) * 4);
      f32x4 rb = *(LAS f32x4*)(sm + K2_R + (k * 64 + c * 8) * 4 + 16);
      LAS char* wb = sm + K2_W + (k & 1) * 12288;
#pragma unroll
      for (int i = 0; i < 3; ++i) {
        u32x2 lo = {pkbf(bflo(v[i][0]) * ra[0], bfhi(v[i][0]) * ra[1]), pkbf(bflo(v[i][1]) * ra[2], bfhi(v[i][1]) * ra[3])};
        u32x2 hi = {pkbf(bflo(v[i][2]) * rb[0], bfhi(v[i][2]) * rb[1]), pkbf(bflo(v[i][3]) * rb[2], bfhi(v[i][3]) * rb[3])};
        *(LAS u32x2*)(wb + wdst[i]) = lo;
        *(LAS u32x2*)(wb + wdst[i] + 256) = hi;
      }
    };
#pragma unroll 1
    for (int k0 = 0; k0 < 32; k0 += PD) {
#pragma unroll
      for (int s = 0; s < PD; ++s) {
        const int k = k0 + s;
        put(wv[s], k);
        if (k + PD < 32) {
#pragma unroll
          for (int i = 0; i < 3; ++i) wv[s][i] = *(const GAS u32x4*)(wsrc[i] + (k + PD) * 128);
        }
        WAITL();
        BAR();
        FENCE();
      }
    }
  }
}

DEVI void k2_conv(LAS char* sm0, int cid, const u16* dsk, char* ws) {
  const int tid = threadIdx.x, lane = tid & 63;
  const int w = __builtin_amdgcn_readfirstlane(tid >> 6);
  LAS char* sm = sm0 + w * CV_SLOT;
  const int r = lane & 15, q = lane >> 4;
  if (w < 4) __builtin_amdgcn_s_setprio(1);
  if (lane < 32) *(LAS u32*)(sm + 4096 + lane * 4) = 0;
#pragma unroll
  for (int i = 0; i < 3; ++i) {
    const int o = lane + 64 * i;
    if (o < 129) *(LAS u32*)(sm + 8320 + o * 4) = 0;
    if (o < 135) *(LAS u32*)(sm + 12932 + o * 4) = 0;
  }
  const int fb = (r & 1) * CV_R1 + 2 * (2044 - 4 * (r >> 1) + 8 * q) - 3584;
  const int gb = ((r & 1) ? CV_U0 : CV_U2) + 64 * (r >> 1) + 16 * q;
#pragma unroll 1
  for (int c = w; c < 12; c += 8) {
    const int d = cid * 12 + c;
    if (d >= D) break;
    const char* kr = ws + OFF_KR + (size_t)d * 8192 + lane * 16;
    const char* ut = ws + OFF_UT + (size_t)d * 4096 + lane * 16;
    WAITL();
#pragma unroll
    for (int i = 0; i < 4; ++i) {
      glds16(kr + i * 1024, sm + i * 1024);
      glds16(kr + 4096 + i * 1024, sm + CV_R1 + i * 1024);
      glds16(ut + i * 1024, sm + CV_U2 + 4 + i * 1024);
      glds16(ut + i * 1024, sm + CV_U0 + i * 1024);
    }
    const float dk = bf1(dsk[d]);
    WAITV(0);
    f32x4 acc[8];
#pragma unroll
    for (int n = 0; n < 8; ++n) acc[n] = f32x4{0, 0, 0, 0};
#pragma unroll 1
    for (int rho = 0; rho < 8; ++rho) {
      u32x4 Ff[8], G[8];
      LAS char* fp = sm + fb - 64 * rho;
      LAS char* gp = sm + gb - 64 * rho;
#pragma unroll
      for (int i = 0; i < 8; ++i) {
        u32x2 lo = ld64(fp + (7 - i) * 512), hi = ld64(fp + (7 - i) * 512 + 8);
        Ff[i] = u32x4{lo[0], lo[1], hi[0], hi[1]};
        G[i] = ld128(gp + i * 512);
      }
#pragma unroll
      for (int n = 0; n < 8; ++n)
#pragma unroll
        for (int mu = 0; mu <= n; ++mu) acc[n] = mma(Ff[mu], G[n - mu], acc[n]);
    }
    const int l0 = 32 * (r >> 1) + 2 * (r & 1) + 8 * q;
#pragma unroll
    for (int n = 0; n < 8; ++n) {
      u32 u0 = *(LAS u32*)(sm + CV_U0 + (256 * n + l0) * 2);
      u32 u1 = *(LAS u32*)(sm + CV_U0 + (256 * n + l0) * 2 + 8);
      u32 y0 = pkbf(acc[n][0], acc[n][1]), y1 = pkbf(acc[n][2], acc[n][3]);
      u32 s0 = pkbf(bflo(u0) * dk, bfhi(u0) * dk), s1 = pkbf(bflo(u1) * dk, bfhi(u1) * dk);
      u32 w0 = pkbf(bflo(y0) + bflo(s0), bfhi(y0) + bfhi(s0));
      u32 w1 = pkbf(bflo(y1) + bflo(s1), bfhi(y1) + bfhi(s1));
      u32 give = (r & 1) ? w0 : w1;
      u32 got = dppu<0xB1>(give);
      u32x2 o = (r & 1) ? u32x2{got, w1} : u32x2{w0, got};
      *(GAS u32x2*)(ws + OFF_YT + (size_t)d * 4096 + (256 * n + 32 * (r >> 1) + 8 * q + 4 * (r & 1)) * 2) = o;
    }
  }
}

__global__ __launch_bounds__(512, 2) void k2(const u16* wsp, const u16* bsp, const u16* dsk, char* ws) {
  extern __shared__ __attribute__((aligned(16))) char smem[];
  LAS char* sm = (LAS char*)smem;
  const int b = blockIdx.x, xcd = b & 7, idx = b >> 3;
  if (idx < 25) {
    const int id = xcd * 25 + idx;
    if (id >= 198) return;
    const int g = id / 54, rem = id - g * 54;
    const int rows = g < 3 ? 6 : 4;
    const int ct = rem / rows;
    k2_gemm(sm, g * 6 + (rem - ct * rows), ct, wsp, bsp, ws);
  } else {
    const int cid = xcd * 7 + (idx - 25);
    if (cid >= 54) return;
    k2_conv(sm, cid, dsk, ws);
  }
}

constexpr int K3_Y = 135 * 1024, K3_X = K3_Y + 10240, K3_LDS = K3_X + 10240;

__global__ __launch_bounds__(512, 2) void k3(const u16* x, char* ws, u16* out) {
  extern __shared__ __attribute__((aligned(16))) char smem[];
  LAS char* sm = (LAS char*)smem;
  const int b = blockIdx.x, xcd = b & 7, idx = b >> 3;
  const int rt = xcd * 4 + (idx & 3), ct = idx >> 2;
  const int tid = threadIdx.x, lane = tid & 63;
  const int w = __builtin_amdgcn_readfirstlane(tid >> 6);
  const int r = lane & 15, q = lane >> 4;
  if (w >= 4) {
    const int lw = w - 4;
#pragma unroll
    for (int i = 0; i < 34; ++i) {
      const int id = min(lw + 4 * i, 134);
      const int fc = id / 5, df = id - fc * 5;
      glds16(ws + OFF_WOF + (size_t)((ct * 5 + df) * 27 + fc) * 1024 + lane * 16, sm + id * 1024);
    }
#pragma unroll
    for (int i = 0; i < 5; ++i) {
      const int j = lw * 5 + i;
      if (j < 10) {
        const int row = j * 8 + (lane >> 3);
        glds16(ws + OFF_YT + (size_t)(ct * 80 + row) * 4096 + rt * 128 + (((lane & 7) ^ ((row >> 1) & 7)) * 16), sm + K3_Y + j * 1024);
      } else {
        const int e = (j - 10) * 64 + lane, row = e / 10, ch = e - row * 10;
        glds16((const char*)x + (size_t)(rt * 64 + row) * (D * 2) + ct * 160 + ch * 16, sm + K3_X + (j - 10) * 1024);
      }
    }
    WAITV(34); BAR();
    WAITV(29); BAR();
    WAITV(24); BAR();
    WAITV(19); BAR();
    WAITV(14); BAR();
    WAITV(9); BAR();
    WAITV(5); BAR();
    WAITV(0); BAR();
  } else {
    u32x4 hf[27];
    const char* hs = ws + OFF_HF + (size_t)((rt * 4 + w) * 27) * 1024 + lane * 16;
#pragma unroll
    for (int i = 0; i < 27; ++i) hf[i] = *(const GAS u32x4*)(hs + i * 1024);
    f32x4 acc[5];
#pragma unroll
    for (int i = 0; i < 5; ++i) acc[i] = f32x4{0, 0, 0, 0};
#pragma unroll
    for (int g = 0; g < 7; ++g) {
      BAR();
      FENCE();
#pragma unroll
      for (int j = 0; j < 4; ++j) {
        const int fc = 4 * g + j;
        if (fc < 27) {
#pragma unroll
          for (int df = 0; df < 5; ++df) acc[df] = mma(hf[fc], ld128(sm + (fc * 5 + df) * 1024 + lane * 16), acc[df]);
        }
      }
    }
    BAR();
    FENCE();
#pragma unroll
    for (int df = 0; df < 5; ++df) {
      const int row = df * 16 + r;
      u32x2 y = ld64(sm + K3_Y + row * 128 + (((2 * w + (q >> 1)) ^ ((row >> 1) & 7)) * 16) + (q & 1) * 8);
      LAS char* xp = sm + K3_X + (16 * w + 4 * q) * 160 + row * 2;
      float xv[4], yv[4] = {bflo(y[0]), bfhi(y[0]), bflo(y[1]), bfhi(y[1])};
#pragma unroll
      for (int i = 0; i < 4; ++i) xv[i] = bf1(*(LAS u16*)(xp + i * 160));
#pragma unroll
      for (int i = 0; i < 4; i += 2) {
        u32 s = pkbf(xv[i] + yv[i], xv[i + 1] + yv[i + 1]);
        u32 bq = pkbf(acc[df][i], acc[df][i + 1]);
        u32 o = pkbf(bflo(s) + bflo(bq), bfhi(s) + bfhi(bq));
        *(LAS u16*)(xp + i * 160) = (u16)o;
        *(LAS u16*)(xp + (i + 1) * 160) = (u16)(o >> 16);
      }
    }
    WAITL();
#pragma unroll
    for (int i = 0; i < 3; ++i) {
      const int e = i * 64 + lane;
      if (e < 160) {
        const int row = e / 10, ch = e - row * 10;
        u32x4 v = ld128(sm + K3_X + (16 * w) * 160 + e * 16);
        *(GAS u32x4*)((char*)out + (size_t)(rt * 64 + 16 * w + row) * (D * 2) + ct * 160 + ch * 16) = v;
      }
    }
  }
}

extern "C" void kernel_launch(void* const* d_in, const int* in_sizes, int n_in, void* d_out, int out_size, void* d_ws,
                              size_t ws_size, hipStream_t stream) {
  const u16* x = (const u16*)d_in[0];
  const u16* lar = (const u16*)d_in[1];
  const u16* aim = (const u16*)d_in[2];
  const u16* cre = (const u16*)d_in[3];
  const u16* cim = (const u16*)d_in[4];
  const u16* ldt = (const u16*)d_in[5];
  const u16* dsk = (const u16*)d_in[6];
  const u16* win = (const u16*)d_in[8];
  const u16* wsp = (const u16*)d_in[9];
  const u16* bsp = (const u16*)d_in[10];
  const u16* wout = (const u16*)d_in[11];
  char* ws = (char*)d_ws;
  (void)hipFuncSetAttribute((const void*)k1, hipFuncAttributeMaxDynamicSharedMemorySize, K1_LDS);
  (void)hipFuncSetAttribute((const void*)k2, hipFuncAttributeMaxDynamicSharedMemorySize, K2_LDS);
  (void)hipFuncSetAttribute((const void*)k3, hipFuncAttributeMaxDynamicSharedMemorySize, K3_LDS);
  k1<<<256, 512, K1_LDS, stream>>>(x, lar, aim, cre, cim, ldt, win, wsp, wout, ws);
  k2<<<256, 512, K2_LDS, stream>>>(wsp, bsp, dsk, ws);
  k3<<<256, 512, K3_LDS, stream>>>(x, ws, (u16*)d_out);
}
```

```cpp
#include <hip/hip_runtime.h>
#include <stdint.h>

typedef unsigned short u16;
typedef unsigned int u32;
typedef __attribute__((ext_vector_type(8))) __bf16 bf16x8;
typedef __attribute__((ext_vector_type(2))) __bf16 bf16x2;
typedef __attribute__((ext_vector_type(8))) _Float16 f16x8;
typedef __attribute__((ext_vector_type(2))) _Float16 f16x2;
typedef __attribute__((ext_vector_type(4))) float f32x4;
typedef __attribute__((ext_vector_type(2))) float f32x2;
typedef __attribute__((ext_vector_type(4))) u32 u32x4;
typedef __attribute__((ext_vector_type(2))) u32 u32x2;
#define LAS __attribute__((address_space(3)))
#define GAS __attribute__((address_space(1)))
#define DEVI __device__ __forceinline__

constexpr int ZS = 67584, K3W = 12, FS = 128, WOE = 5, K3S = 2, PSL = 16, K3YF = 21, WE = 6, GSL = 4, P0 = 4, GL = 24;
constexpr int L = 2048, D = 640, F = 1728, FH = 864, NM = 128;

constexpr size_t OFF_Z2F = 0;
constexpr size_t OFF_Z1A = OFF_Z2F + 54 * ZS;
constexpr size_t OFF_ST = OFF_Z1A + 128 * 54 * 512;
constexpr size_t OFF_EP = OFF_ST + 32 * 8192;
constexpr size_t OFF_CNT = OFF_EP + 4096;
constexpr size_t OFF_F1 = OFF_CNT + 65536;
constexpr size_t OFF_F0 = OFF_F1 + 32768;
constexpr size_t OFF_F2 = OFF_F0 + 32768;
constexpr size_t OFF_UT = OFF_F2 + 32768;
constexpr size_t OFF_KR = OFF_UT + 640 * 4096;
constexpr size_t OFF_WOF = OFF_KR + 640 * 4096;
constexpr size_t OFF_HF = OFF_WOF + 40 * 27 * 1024;
constexpr size_t OFF_YT = OFF_HF + 128 * 27 * 1024;

DEVI float bflo(u32 v) { return __uint_as_float(v << 16); }
DEVI float bfhi(u32 v) { return __uint_as_float(v & 0xffff0000u); }
DEVI f32x2 unpk(u32 v) { return f32x2{bflo(v), bfhi(v)}; }
DEVI float bf1(u16 v) { return __uint_as_float((u32)v << 16); }
DEVI u32 pkbf(f32x2 x) { return __builtin_bit_cast(u32, __builtin_convertvector(x, bf16x2)); }
DEVI u32 pkbf(float a, float b) { return pkbf(f32x2{a, b}); }
DEVI u32 pkf16(f32x2 x) { return __builtin_bit_cast(u32, __builtin_convertvector(x, f16x2)); }
DEVI float rbf(float a) { return bflo(pkbf(a, a)); }
DEVI f32x2 fma2(f32x2 a, f32x2 b, f32x2 c) { return __builtin_elementwise_fma(a, b, c); }
DEVI float dot2(u32 a, u32 b, float c) {
  return __builtin_amdgcn_fdot2_f32_bf16(__builtin_bit_cast(bf16x2, a), __builtin_bit_cast(bf16x2, b), c, false);
}
DEVI f32x4 mma(u32x4 a, u32x4 b, f32x4 c) {
  return __builtin_amdgcn_mfma_f32_16x16x32_bf16(__builtin_bit_cast(bf16x8, a), __builtin_bit_cast(bf16x8, b), c, 0, 0, 0);
}
DEVI f32x4 mmah(u32x4 a, u32x4 b, f32x4 c) {
  return __builtin_amdgcn_mfma_f32_16x16x32_f16(__builtin_bit_cast(f16x8, a), __builtin_bit_cast(f16x8, b), c, 0, 0, 0);
}
DEVI void glds16(const void* sbase, u32 voff, u32 lds) {
  asm volatile("s_mov_b32 m0, %2\n\ts_nop 0\n\tglobal_load_lds_dwordx4 %0, %1" ::"v"(voff), "s"(sbase), "s"(lds) : "memory");
}
DEVI void glds4(const void* sbase, u32 voff, u32 lds) {
  asm volatile("s_mov_b32 m0, %2\n\ts_nop 0\n\tglobal_load_lds_dword %0, %1" ::"v"(voff), "s"(sbase), "s"(lds) : "memory");
}
#define SETM0(lds) asm volatile("s_mov_b32 m0, %0\n\ts_nop 0" ::"s"(lds) : "memory")
#define GLDS16(sbase, voff, off) asm volatile("global_load_lds_dwordx4 %0, %1 offset:%2" ::"v"(voff), "s"(sbase), "i"(off) : "memory")
#define GLDS16C(sbase, voff, off) asm volatile("global_load_lds_dwordx4 %0, %1 offset:%2 sc1" ::"v"(voff), "s"(sbase), "i"(off) : "memory")
#define GLD128(dst, voff, sbase, off) asm volatile("global_load_dwordx4 %0, %1, %2 offset:%3" : "=v"(dst) : "v"(voff), "s"(sbase), "i"(off))
template <class T> DEVI void sts(u32 a, T v) { *(LAS T*)(uintptr_t)a = v; }
template <class T> DEVI T lds(u32 a) { return *(LAS T*)(uintptr_t)a; }
DEVI u32x4 ld128(u32 a) { return lds<u32x4>(a); }
template <int CTRL> DEVI float dppf(float v) {
  return __int_as_float(__builtin_amdgcn_update_dpp(0, __float_as_int(v), CTRL, 0xf, 0xf, true));
}
template <int CTRL> DEVI u32 dppu(u32 v) { return (u32)__builtin_amdgcn_update_dpp(0, (int)v, CTRL, 0xf, 0xf, true); }
DEVI float sum8(float v) {
  v += dppf<0xB1>(v);
  v += dppf<0x4E>(v);
  v += dppf<0x141>(v);
  return v;
}
#define WAITV(n) asm volatile("s_waitcnt vmcnt(%0)" ::"n"(n) : "memory")
#define WAITL() asm volatile("s_waitcnt lgkmcnt(0)" ::: "memory")
#define WAITLN(n) asm volatile("s_waitcnt lgkmcnt(%0)" ::"n"(n) : "memory")
#define BAR() __builtin_amdgcn_s_barrier()
#define FENCE() asm volatile("" ::: "memory")
#define SCHED() __builtin_amdgcn_sched_barrier(0)
#define DSR128(dst, addr, off) asm volatile("ds_read_b128 %0, %1 offset:%2" : "=v"(dst) : "v"(addr), "i"(off))
#define DSR64(dst, addr, off) asm volatile("ds_read_b64 %0, %1 offset:%2" : "=v"(dst) : "v"(addr), "i"(off))
#define DSRTR(dst, addr, off) asm volatile("ds_read_b64_tr_b16 %0, %1 offset:%2" : "=v"(dst) : "v"(addr), "i"(off))
#define DSR32(dst, addr, off) asm volatile("ds_read_b32 %0, %1 offset:%2" : "=v"(dst) : "v"(addr), "i"(off))
#define DSW32(addr, val, off) asm volatile("ds_write_b32 %0, %1 offset:%2" ::"v"(addr), "v"(val), "i"(off) : "memory")
#define MMA(acc, a, b) asm volatile("v_mfma_f32_16x16x32_bf16 %0, %1, %2, %0" : "+v"(acc) : "v"(a), "v"(b))
#define MMA_DRAIN() do { asm volatile("s_nop 7\n\ts_nop 1" ::: "memory"); SCHED(); } while (0)
DEVI void gst128(void* sbase, u32 voff, u32x4 v) {
  asm volatile("global_store_dwordx4 %0, %1, %2" " sc1\n\ts_nop 1" ::"v"(voff), "v"(v), "s"(sbase) : "memory");
}
#define PINV(x) asm volatile("" : "+v"(x))
DEVI float opq(float x) { asm("" : "+v"(x)); return x; }
DEVI int lane_id() {
  int l;
  asm volatile("v_mbcnt_lo_u32_b32 %0, -1, 0\n\tv_mbcnt_hi_u32_b32 %0, -1, %0" : "=v"(l));
  __builtin_assume(l >= 0 && l < 64);
  return l;
}
DEVI int rfl(u32 v) { return __builtin_amdgcn_readfirstlane((int)v); }
DEVI void lds_wait(u32 addr) {
  for (int guard = 0; guard < (1 << 20); ++guard) {
    u32 v;
    asm volatile("ds_read_b32 %0, %1\n\ts_waitcnt lgkmcnt(0)" : "=v"(v) : "v"(addr) : "memory");
    if (rfl(v) != 0) break;
    __builtin_amdgcn_s_sleep(1);
  }
}
DEVI u32x4 cat(u32x2 a, u32x2 b) { return u32x4{a[0], a[1], b[0], b[1]}; }
template <int V> struct IC { static constexpr int value = V; };
template <int I, int N, class Fn> DEVI void static_for(Fn&& f) {
  if constexpr (I < N) { f(IC<I>{}); static_for<I + 1, N>(f); }
}

DEVI f32x2 gelu2(f32x2 x) {
  const float c1 = -2.0f * 0.7978845608028654f * 1.4426950408889634f;
  const float c2 = c1 * 0.044715f;
  f32x2 t = x * fma2(x * x, f32x2{c2, c2}, f32x2{c1, c1});
  f32x2 e = f32x2{__builtin_amdgcn_exp2f(t[0]), __builtin_amdgcn_exp2f(t[1])} + f32x2{1.0f, 1.0f};
  return x * f32x2{__builtin_amdgcn_rcpf(e[0]), __builtin_amdgcn_rcpf(e[1])};
}

constexpr int LDS_BYTES = 163840, C_TAG = LDS_BYTES - 64, C_N1 = C_TAG + 8, C_N2 = C_TAG + 12, C_XF = C_TAG + 16, C_RDY = C_TAG + 32, C_N3 = C_TAG + 36, C_N0 = C_TAG + 40, C_N4 = C_TAG + 44;
DEVI void read_epoch(u32x2& ep, char* ws) { asm volatile("global_load_dwordx2 %0, %1, %2 sc1" : "=v"(ep) : "v"(0), "s"(ws + OFF_EP)); }
template <int YOUNGER = 0> DEVI void arrive(u32 cnt, int nst, char* flag) {
  WAITV(YOUNGER);
  if (lane_id() == 0 && __hip_atomic_fetch_add((LAS u32*)(uintptr_t)cnt, 1u, __ATOMIC_RELAXED, __HIP_MEMORY_SCOPE_WORKGROUP) == (u32)nst - 1) {
    FENCE();
    __hip_atomic_store((GAS unsigned long long*)flag, *(volatile LAS unsigned long long*)(uintptr_t)C_TAG, __ATOMIC_RELAXED, __HIP_MEMORY_SCOPE_AGENT);
  }
}
DEVI bool look_flags(const char* base, u32 off) {
  unsigned long long v;
  asm volatile("global_load_dwordx2 %0, %1, %2 sc1\n\ts_waitcnt vmcnt(0)" : "=v"(v) : "v"(off), "s"(base) : "memory");
  return __all(v == lds<unsigned long long>(C_TAG));
}
DEVI void wait_flags(const char* base, u32 off) {
#pragma unroll 1
  for (int guard = 0; guard < (1 << 18) && !look_flags(base, off); ++guard) __builtin_amdgcn_s_sleep(PSL);
}
DEVI int wave2() { return __builtin_amdgcn_readfirstlane(threadIdx.x >> 6); }
DEVI u32 wo_flag(int fc) {
  const int n = ((blockIdx.x >> 3) & 7) * 27 + fc, id = (n & 1) * 28 + (n >> 3);
  return ((((n & 7) >> 1) * 8 + (id & 7)) * 8 + (id >> 3)) * 128;
}

DEVI void k2_region(int x, int& r0, int& nr, int& c0, int& nc) {
  const int p = x & 3;
  nr = (p == 1 || p == 2) ? 6 : 5;
  nc = 10 - nr;
  r0 = (x >> 2) * 11 + (p < 2 ? 0 : 11 - nr);
  c0 = (p & 1) ? 9 - nc : 0;
}

constexpr int K1_RING = 0;
constexpr int K1_A = 65536;
constexpr int K1_RDY = C_RDY, K1_SCR = 147456, K1_STAT = K1_SCR + 1024, K1_PART = K1_SCR + 1536;
constexpr int K1_KG = K1_PART + 2048;
static_assert(12 * 12800 + 4096 + 256 <= C_TAG && K1_KG + 8192 <= C_TAG, "lds");

DEVI f32x2 cmul_a(f32x2 a, f32x2 z) {
  f32x2 t;
  asm("v_pk_mul_f32 %0, %1, %2 op_sel:[1,1] op_sel_hi:[1,0] neg_lo:[0,1]" : "=v"(t) : "v"(a), "v"(z));
  return t;
}
DEVI f32x2 cexp_p(float lr2, float wv, float p) {
  const float e = __builtin_amdgcn_exp2f(lr2 * p);
  const float ph = __builtin_amdgcn_fractf(wv * p);
  return f32x2{e, e} * f32x2{__builtin_amdgcn_cosf(ph), __builtin_amdgcn_sinf(ph)};
}
template <int CTRL> DEVI u32 dppmax(u32 v) { return max(v, dppu<CTRL>(v)); }
constexpr int KT = 388 * 4;

struct KgPar { const u16 *lar, *aim, *cre, *cim, *ldt, *dsk; };
struct KgReg { u32 dt, dk, cr, ci, la, ai; };
DEVI void kg_load(KgReg& k, const KgPar& p, int d, int lane) {
  asm volatile("global_load_ushort %0, %1, %2" : "=v"(k.dt) : "v"(0), "s"(p.ldt + d));
  asm volatile("global_load_ushort %0, %1, %2" : "=v"(k.dk) : "v"(0), "s"(p.dsk + d));
  asm volatile("global_load_dword %0, %1, %2" : "=v"(k.cr) : "v"(lane * 4), "s"(p.cre + d * NM));
  asm volatile("global_load_dword %0, %1, %2" : "=v"(k.ci) : "v"(lane * 4), "s"(p.cim + d * NM));
  asm volatile("global_load_dword %0, %1, %2" : "=v"(k.la) : "v"(lane * 4), "s"(p.lar + d * NM));
  asm volatile("global_load_dword %0, %1, %2" : "=v"(k.ai) : "v"(lane * 4), "s"(p.aim + d * NM));
}
#define KG_WAIT(k) asm volatile("s_waitcnt vmcnt(0)" : "+v"(k.dt), "+v"(k.dk), "+v"(k.cr), "+v"(k.ci), "+v"(k.la), "+v"(k.ai)::"memory")

template <class After> DEVI void kgen(const KgPar& kp, KgReg k, int d0, int d1, u32 sm, int lane, char* ws, After after) {
  const int r = lane & 15, q = lane >> 4;
  u32 wr[16], rd[4], t1[4];
#pragma unroll
  for (int i = 0; i < 16; ++i) wr[i] = sm + ((lane * 8) ^ (i << 4));
#pragma unroll
  for (int s = 0; s < 4; ++s) rd[s] = sm + r * 512 + (((4 * s + q) ^ r) * 16);
  const u32 t0 = sm + (q & 1) * 4 * KT + (q >> 1) * 64 + 4 * r, t2 = sm + 512 + q * 256 + 4 * r;
#pragma unroll
  for (int i = 0; i < 4; ++i) t1[i] = sm + 4 * r + (q < 2 ? 128 + q * 64 + i * 2 * KT : q * 128 + (i >> 1) * 64 + (i & 1) * 4 * KT);
  const u32 gb = sm + 4 * (63 - lane);
  KG_WAIT(k);
#pragma unroll 1
  for (int d = d0; d < d1; d += 4) {
    const float dt = rbf(__builtin_amdgcn_exp2f(bflo(k.dt) * 1.4426950408889634f));
    const float dskip = lane == 0 ? bflo(k.dk) : 0.f;
    u32 mx = max(max(k.cr & 0x7fff0000u, k.ci & 0x7fff0000u), max((k.cr << 16) & 0x7fff0000u, (k.ci << 16) & 0x7fff0000u));
    mx = dppmax<0x140>(dppmax<0x141>(dppmax<0x4E>(dppmax<0xB1>(mx))));
    const u32 mxs = max(max((u32)__builtin_amdgcn_readlane(mx, 0), (u32)__builtin_amdgcn_readlane(mx, 16)),
                        max((u32)__builtin_amdgcn_readlane(mx, 32), (u32)__builtin_amdgcn_readlane(mx, 48)));
    const u32 ex = min(max(mxs >> 23, 27u), 254u);
    const float cup = __uint_as_float((267u - ex) << 23), cdn = __uint_as_float((ex - 26u) << 23);
    const float dl = dt * 1.4426950408889634f, dw = dt * 0.15915494309189535f;
    f32x2 z1[2], Z[2], w8[2], a[2][8], b[2], e[2];
#pragma unroll
    for (int m = 0; m < 2; ++m) {
      const float la = m ? bfhi(k.la) : bflo(k.la), ai = m ? bfhi(k.ai) : bflo(k.ai);
      const float lr2 = -rbf(__builtin_amdgcn_exp2f(la * 1.4426950408889634f)) * dl, wv = ai * dw;
      z1[m] = cexp_p(lr2, wv, 1.f);
      Z[m] = cexp_p(lr2, wv, 128.f);
      w8[m] = cexp_p(lr2, -wv, 8.f);
      e[m] = cexp_p(lr2, wv, 1024.f);
      a[m][0] = (m ? f32x2{bfhi(k.cr), bfhi(k.ci)} : f32x2{bflo(k.cr), bflo(k.ci)}) * cup;
      b[m] = f32x2{16384.0f, 0.f};
    }
    SCHED();
    kg_load(k, kp, d + 4 < d1 ? d + 4 : d, lane);
    {
      const f32x2 u0 = cmul_a(a[0][0], e[0]), u1 = cmul_a(a[1][0], e[1]);
      SCHED();
      asm volatile("s_nop 0");
      e[0] = fma2(f32x2{a[0][0][0], a[0][0][0]}, e[0], u0); e[1] = fma2(f32x2{a[1][0][0], a[1][0][0]}, e[1], u1);
    }
    u32x2 pk[2][2];
#define PUT2(j, v0, v1) asm volatile("ds_write2st64_b64 %0, %1, %2 offset0:%3 offset1:%4" ::"v"(wr[j]), "v"(v0), "v"(v1), "i"(j), "i"((j) + 16) : "memory")
#define PUT1(j, v0) asm volatile("ds_write_b64 %0, %1 offset:%2" ::"v"(wr[j]), "v"(v0), "i"((j) * 512) : "memory")
#define KSTEP(P, D1, S1, Z1, D2, S2, Z2, PACK2, PUTS)                                                         \
    {                                                                                                         \
      f32x2 t_[4] = {cmul_a(S1(0), Z1[0]), cmul_a(S1(1), Z1[1]), cmul_a(S2(0), Z2[0]), cmul_a(S2(1), Z2[1])}; \
      SCHED(); PUTS; SCHED();                                                                                 \
      D1(0) = fma2(f32x2{S1(0)[0], S1(0)[0]}, Z1[0], t_[0]); SCHED();                                         \
      D1(1) = fma2(f32x2{S1(1)[0], S1(1)[0]}, Z1[1], t_[1]); SCHED();                                         \
      D2(0) = fma2(f32x2{S2(0)[0], S2(0)[0]}, Z2[0], t_[2]); SCHED();                                         \
      D2(1) = fma2(f32x2{S2(1)[0], S2(1)[0]}, Z2[1], t_[3]); SCHED();                                         \
      pk[P][0] = u32x2{pkf16(D1(0)), pkf16(D1(1))};                                                           \
      if (PACK2) pk[P][1] = u32x2{pkf16(D2(0)), pkf16(D2(1))};                                                \
      asm volatile("" : "+v"(pk[P][0]), "+v"(pk[P][1]) : "v"(pk[(P) ^ 1][0]), "v"(pk[(P) ^ 1][1]));           \
      SCHED();                                                                                                \
    }
#define B_(m) b[m]
#define E_(m) e[m]
    pk[0][0] = u32x2{pkf16(b[0]), pkf16(b[1])}; pk[0][1] = u32x2{pkf16(e[0]), pkf16(e[1])};
    asm volatile("" : "=v"(pk[1][0]), "=v"(pk[1][1]));
    SCHED();
#pragma unroll
    for (int j = 1; j < 9; ++j) KSTEP(j & 1, B_, B_, w8, E_, E_, Z, true, PUT2(j - 1, pk[~j & 1][0], pk[~j & 1][1]))
#define AD_(m) a[m][j - 8]
#define AS_(m) a[m][j - 9]
#pragma unroll
    for (int j = 9; j < 16; ++j)
      KSTEP(j & 1, B_, B_, w8, AD_, AS_, z1, false, if (j == 9) PUT2(8, pk[0][0], pk[0][1]); else PUT1(j - 1, pk[~j & 1][0]))
    PUT1(15, pk[1][0]);
    u32x4 bf[8], af[8];
#pragma unroll
    for (int s = 0; s < 8; ++s) { DSR128(bf[s], rd[s & 3], (s >> 2) * 256); DSR128(af[s], rd[s & 3], 8192 + (s >> 2) * 256); }
#define A1_(m) a[m][i1]
#define A2_(m) a[m][i2]
#pragma unroll
    for (int j = 0; j < 4; ++j) {
      const int i1 = 2 * j, i2 = 2 * j + 1;
      u32x2 q1 = {pkf16(a[0][i1]), pkf16(a[1][i1])}, q2 = {pkf16(a[0][i2]), pkf16(a[1][i2])};
      SCHED();
      KSTEP(j & 1, A1_, A1_, Z, A2_, A2_, Z, true,
            if (j > 0) { PUT1(6 + 2 * j, pk[~j & 1][0]); PUT1(7 + 2 * j, pk[~j & 1][1]); } PUT1(i1, q1); PUT1(i2, q2))
      asm volatile("" ::"v"(q1), "v"(q2));
    }
    PUT1(14, pk[1][0]); PUT1(15, pk[1][1]);
    f32x4 acc[3];
    asm volatile("s_waitcnt lgkmcnt(15)" : "+v"(af[0]), "+v"(af[1]), "+v"(af[2]), "+v"(af[3]), "+v"(af[4]), "+v"(af[5]), "+v"(af[6]), "+v"(af[7]),
                 "+v"(bf[0]), "+v"(bf[1]), "+v"(bf[2]), "+v"(bf[3]), "+v"(bf[4]), "+v"(bf[5]), "+v"(bf[6]), "+v"(bf[7]));
    acc[2] = mmah(af[0], bf[0], f32x4{0, 0, 0, 0});
#pragma unroll
    for (int s = 1; s < 8; ++s) acc[2] = mmah(af[s], bf[s], acc[2]);
    asm volatile("" : "+v"(acc[2]));
#pragma unroll
    for (int s = 0; s < 8; ++s) DSR128(af[s], rd[s & 3], (s >> 2) * 256);
    SCHED();
#pragma unroll
    for (int j = 0; j < 8; ++j) {
      const int i1 = j < 4 ? 4 * (j & 1) : 0, i2 = j < 4 ? 4 * (j & 1) + 2 : 4;
      KSTEP(j & 1, A1_, A1_, Z, A2_, A2_, Z, true, if (j > 0) { PUT1(2 * j - 2, pk[~j & 1][0]); PUT1(2 * j - 1, pk[~j & 1][1]); })
    }
    PUT1(14, pk[1][0]); PUT1(15, pk[1][1]);
    SCHED();
    u32x4 a1[8];
#pragma unroll
    for (int s = 0; s < 8; ++s) DSR128(a1[s], rd[s & 3], (s >> 2) * 256);
    asm volatile("s_waitcnt lgkmcnt(15)" : "+v"(af[0]), "+v"(af[1]), "+v"(af[2]), "+v"(af[3]), "+v"(af[4]), "+v"(af[5]), "+v"(af[6]), "+v"(af[7]));
    acc[0] = mmah(af[0], bf[0], f32x4{0, 0, 0, 0});
#pragma unroll
    for (int s = 1; s < 8; ++s) acc[0] = mmah(af[s], bf[s], acc[0]);
    asm volatile("s_waitcnt lgkmcnt(0)" : "+v"(a1[0]), "+v"(a1[1]), "+v"(a1[2]), "+v"(a1[3]), "+v"(a1[4]), "+v"(a1[5]), "+v"(a1[6]), "+v"(a1[7]), "+v"(acc[0]));
    acc[1] = mmah(a1[0], bf[0], f32x4{0, 0, 0, 0});
#pragma unroll
    for (int s = 1; s < 8; ++s) acc[1] = mmah(a1[s], bf[s], acc[1]);
    const float ks = dt * cdn;
    acc[2] *= ks; acc[0] *= ks; acc[0][0] += dskip; acc[1] *= ks;
#pragma unroll
    for (int i = 0; i < 4; ++i) { DSW32(t2, pkbf(acc[2][i], acc[2][i]), i * 64); DSW32(t0, pkbf(acc[0][i], acc[0][i]), i * KT); }
#pragma unroll
    for (int i = 0; i < 4; ++i) DSW32(t1[i], pkbf(acc[1][i], acc[1][i]), 0);
    KG_WAIT(k);
    u32x2 lh[4];
    u32 mid, pl[8];
#pragma unroll
    for (int i = 0; i < 4; ++i) asm volatile("ds_read2_b32 %0, %1 offset0:%2 offset1:%3" : "=v"(lh[i]) : "v"(gb), "i"((3 - i) * 64), "i"((3 - i) * 64 + 1));
    DSR32(mid, gb, 4 * KT + 256);
#pragma unroll
    for (int i = 1; i < 8; ++i) DSR32(pl[i], gb, i * KT);
    asm volatile("s_waitcnt lgkmcnt(0)" : "+v"(lh[0]), "+v"(lh[1]), "+v"(lh[2]), "+v"(lh[3]), "+v"(mid), "+v"(pl[1]), "+v"(pl[2]), "+v"(pl[3]),
                 "+v"(pl[4]), "+v"(pl[5]), "+v"(pl[6]), "+v"(pl[7])::"memory");
    char* o = ws + OFF_KR + (size_t)d * 4096;
    auto pair = [](u32 lo, u32 hi) { return __builtin_amdgcn_perm(hi, lo, 0x07060100u); };
#pragma unroll
    for (int i = 0; i < 2; ++i)
      gst128(o + i * 1024, lane * 16, u32x4{lh[i][1], pair(lh[i][1], (lane & 1) ? lh[i][0] : lh[i][1]), lh[i][0], lh[i][0]});
    gst128(o + 2048, lane * 16, u32x4{lh[2][1], mid, pair(mid, lh[2][0]), lh[2][0]});
    const bool two = lane < 32;
    gst128(o + 3072, lane * 16, u32x4{pair(two ? lh[3][1] : pl[7], pl[6]), pair(two ? pl[4] : pl[5], pl[4]), pair(two ? pl[4] : pl[3], pl[2]),
                                      pair(two ? lh[3][0] : pl[1], lh[3][0])});
    after(d);
  }
}

struct K1Fr { u32x4 a[2][4]; u32x2 b[2][2][2]; };
struct K1Ad { u32 a[2][2], b[2][2]; };
template <int S, int KC, int T> DEVI void k1_ra(K1Fr& n, const K1Ad& ad) { DSR128(n.a[KC][T], ad.a[KC][T >> 1], (T & 1) * 20480 + S * 1024); }
template <int S, int KC, int J, int H> DEVI void k1_rb(K1Fr& n, const K1Ad& ad) {
  DSRTR(n.b[KC][J][H], ad.b[(S >> 1) & 1][J], (S & 1) * 16384 + KC * 4096 + H * 512);
}
#define K1_M(C, KC, T, J) MMA(acc[T][J], cat(C.b[KC][J][0], C.b[KC][J][1]), C.a[KC][T])
template <int S>
DEVI void k1_step(K1Fr& c, K1Fr& n, const K1Ad& ad, f32x4 (&acc)[4][2], int grp = 0) {
  WAITLN(4);
  if (S < 9 || grp == 0) BAR();
  SCHED();
  K1_M(c, 0, 0, 0); k1_rb<S, 0, 0, 0>(n, ad);
  K1_M(c, 0, 0, 1); k1_rb<S, 0, 0, 1>(n, ad);
  K1_M(c, 0, 1, 0); k1_rb<S, 0, 1, 0>(n, ad);
  K1_M(c, 0, 1, 1); k1_rb<S, 0, 1, 1>(n, ad);
  K1_M(c, 0, 2, 0); k1_rb<S, 1, 0, 0>(n, ad);
  K1_M(c, 0, 2, 1); k1_rb<S, 1, 0, 1>(n, ad);
  K1_M(c, 0, 3, 0); k1_rb<S, 1, 1, 0>(n, ad);
  K1_M(c, 0, 3, 1); k1_rb<S, 1, 1, 1>(n, ad);
  WAITLN(8);
  K1_M(c, 1, 0, 0); k1_ra<S, 0, 0>(n, ad);
  K1_M(c, 1, 0, 1); k1_ra<S, 0, 1>(n, ad);
  K1_M(c, 1, 1, 0); k1_ra<S, 0, 2>(n, ad);
  K1_M(c, 1, 1, 1); k1_ra<S, 0, 3>(n, ad);
  K1_M(c, 1, 2, 0); k1_ra<S, 1, 0>(n, ad);
  K1_M(c, 1, 2, 1); k1_ra<S, 1, 1>(n, ad);
  K1_M(c, 1, 3, 0); k1_ra<S, 1, 2>(n, ad);
  K1_M(c, 1, 3, 1); k1_ra<S, 1, 3>(n, ad);
  SCHED();
}

DEVI void k1_loop(const K1Ad& ad, int grp, f32x4 (&acc)[4][2]) {
  K1Fr fa, fb;
  k1_rb<0, 0, 0, 0>(fa, ad); k1_rb<0, 0, 0, 1>(fa, ad); k1_rb<0, 0, 1, 0>(fa, ad); k1_rb<0, 0, 1, 1>(fa, ad);
  k1_rb<0, 1, 0, 0>(fa, ad); k1_rb<0, 1, 0, 1>(fa, ad); k1_rb<0, 1, 1, 0>(fa, ad); k1_rb<0, 1, 1, 1>(fa, ad);
  k1_ra<0, 0, 0>(fa, ad); k1_ra<0, 0, 1>(fa, ad); k1_ra<0, 0, 2>(fa, ad); k1_ra<0, 0, 3>(fa, ad);
  k1_ra<0, 1, 0>(fa, ad); k1_ra<0, 1, 1>(fa, ad); k1_ra<0, 1, 2>(fa, ad); k1_ra<0, 1, 3>(fa, ad);
  k1_step<1>(fa, fb, ad, acc); k1_step<2>(fb, fa, ad, acc); k1_step<3>(fa, fb, ad, acc);
  k1_step<4>(fb, fa, ad, acc); k1_step<5>(fa, fb, ad, acc); k1_step<6>(fb, fa, ad, acc);
  k1_step<7>(fa, fb, ad, acc); k1_step<8>(fb, fa, ad, acc); k1_step<9>(fa, fb, ad, acc, grp);
  WAITLN(4);
  if (grp == 0) BAR();
  SCHED();
#pragma unroll
  for (int kc = 0; kc < 2; ++kc) {
    if (kc == 1) WAITL();
#pragma unroll
    for (int t = 0; t < 4; ++t)
#pragma unroll
      for (int j = 0; j < 2; ++j) K1_M(fb, kc, t, j);
  }
  SCHED();
  MMA_DRAIN();
}

DEVI float diag4(f32x4 g, int r) {
  const float d01 = (r & 1) ? g[1] : g[0], d23 = (r & 1) ? g[3] : g[2];
  return (r & 2) ? d23 : d01;
}
DEVI f32x2 ln_coef(float s1, float s2, float inv_n) {
  const float mean = s1 * inv_n;
  const float var = fmaxf(s2 * inv_n - mean * mean, 0.f);
  const float ri = __builtin_amdgcn_rcpf(rbf(__builtin_amdgcn_sqrtf(rbf(rbf(var) + 1e-5f))));
  return f32x2{ri, opq(-rbf(mean) * ri)};
}

DEVI void k1_gemm(int rp, int ct, int idx, int bid, const u16* wout, const u16* x, const u16* win, const u16* wsp, char* ws) {
  const int wp = __builtin_amdgcn_readfirstlane(threadIdx.x >> 6);
  const int t0 = rp * 64;
  const int u1 = ct < 6 ? 2 * ct : 12, u2 = ct < 6 ? 15 + 2 * ct : 13;
  if (wp == 0) {
    const int lane = lane_id(), r = lane & 15, q = lane >> 4;
    const int u = min(idx * 8 + (bid & 7), 215), uc = u / 27, fc = u - uc * 27;
    u32x2 ep;
    read_epoch(ep, ws);
    if (lane < 14) sts<u32>(C_N1 + lane * 4, 0);
    WAITL();
    BAR();
#pragma unroll
    for (int i = 0; i < 5; ++i) {
      const int e = i * 64 + lane, row = e / 10;
      glds16((const char*)wout + (size_t)(fc * 32) * (D * 2) + uc * 160, row * (D * 2) + (e - row * 10) * 16, K1_KG + i * 1024);
    }
    BAR(); BAR(); BAR(); BAR();
    const u32 ra = K1_KG + (4 * q + (r >> 2)) * 160 + (r & 3) * 16, rb = K1_KG + (4 * q + (r >> 2)) * 160 + 128 + (r & 3) * 8;
    asm volatile("s_waitcnt vmcnt(0)" : "+v"(ep)::"memory");
    sts<u32x2>(C_TAG, ep + u32x2{1u, 1u});
    u32x2 v[5][2];
#pragma unroll
    for (int f = 0; f < 4; ++f) { DSRTR(v[f][0], ra, (f >> 1) * 64 + (f & 1) * 8); DSRTR(v[f][1], ra, (f >> 1) * 64 + (f & 1) * 8 + 2560); }
    DSRTR(v[4][0], rb, 0); DSRTR(v[4][1], rb, 2560);
    asm volatile("s_waitcnt lgkmcnt(0)" : "+v"(v[0][0]), "+v"(v[0][1]), "+v"(v[1][0]), "+v"(v[1][1]), "+v"(v[2][0]), "+v"(v[2][1]),
                 "+v"(v[3][0]), "+v"(v[3][1]), "+v"(v[4][0]), "+v"(v[4][1]));
#pragma unroll 1
    for (int i = 4; i < 19; ++i) BAR();
    if (idx < 27)
#pragma unroll
    for (int f = 0; f < 5; ++f) gst128(ws + OFF_WOF + (size_t)(u * 5 + f) * 1024, lane * 16, cat(v[f][0], v[f][1]));
    return;
  }
  if (wp < 4) {
    const int lane = lane_id(), prow = lane >> 3, pp = lane & 7;
    const int p0 = wp == 1 ? 0 : wp * 5 - 4;
    if (wp > 1) glds4(win, (u32)min((idx * 8 + (bid & 7)) * 78 + min((wp - 2) * 64 + lane, 77), 17279) * 128u, K1_SCR);
    u32 vo[6];
#pragma unroll
    for (int i = 0; i < 6; ++i) {
      const int p = min(p0 + i, 15);
      vo[i] = (prow + 8 * (p & 7)) * (F * 2) + (p >> 3) * 128 + (((pp >> 1) ^ ((prow >> 1) & 1) ^ ((p & 1) << 1)) * 2 + (pp & 1)) * 16 + (2 - i % 3) * 1024;
    }
    const char* g2 = (const char*)win + u2 * 128 - 2048;
    const char* g1 = (const char*)win + u1 * 128 - 2048 - 640 * (F * 2);
    auto run = [&](auto NP) {
      constexpr int np = decltype(NP)::value;
      auto issue = [&](int s) {
        const char* gs = (s < 10 ? g2 : g1) + s * 64 * (F * 2);
        const u32 v0 = vo[0], v1 = vo[1], v2 = vo[2], v3 = vo[3], v4 = vo[4], v5 = vo[5];
        SETM0(K1_RING + (s & 3) * 16384 + p0 * 1024);
        GLDS16(gs, v0, 0); GLDS16(gs, v1, 1024); GLDS16(gs, v2, 2048);
        SETM0(K1_RING + (s & 3) * 16384 + p0 * 1024 + 3072);
        GLDS16(gs, v3, 0); GLDS16(gs, v4, 1024);
        if (np == 6) GLDS16(gs, v5, 2048);
      };
      BAR();
      issue(0); issue(1); issue(2); issue(3);
#pragma unroll
      for (int i = 0; i < 2; ++i) {
        const int e = ((3 - wp) + 3 * i) * 64;
        if (e < 8 * GL) glds4(wsp, (u32)(bid * 8 + (lane >> 3)) * 4096u + (e / 8 + (lane & 7)) * 128u, K1_SCR);
      }
      WAITV(3 * np);
      BAR();
#pragma unroll 1
      for (int s = 0; s < 16; ++s) {
        WAITV(2 * np);
        BAR();
        issue(s + 4);
      }
      WAITV(2 * np); BAR();
      WAITV(0); BAR();
    };
    if (wp == 1) run(IC<6>{});
    else run(IC<5>{});
    return;
  }
  const int lane = lane_id(), prow = lane >> 3, pp = lane & 7, r = lane & 15, q = lane >> 4;
  const int w = wp - 4, g = w & 3, grp = w >> 2;
  u32x4 c[10];
  {
    const char* gx = (const char*)x + (size_t)(t0 + 8 * w) * (D * 2);
    const u32 vo = prow * (D * 2) + ((pp ^ prow) * 16);
#pragma unroll
    for (int j = 0; j < 10; ++j) GLD128(c[j], vo, gx, j * 128);
  }
  BAR();
  K1Ad ad;
  {
    const u32 aE = K1_A + (r >> 3) * 10240 + (r & 7) * 128 + ((q ^ (r & 7)) * 16);
    ad.a[0][0] = aE; ad.a[0][1] = aE + 40960; ad.a[1][0] = aE ^ 64; ad.a[1][1] = (aE ^ 64) + 40960;
    const int brow = 8 * q + (r >> 2);
    const int bs = ((brow >> 1) & 1) | (((brow >> 3) & 1) << 1);
#pragma unroll
    for (int j = 0; j < 2; ++j) {
      ad.b[0][j] = K1_RING + grp * 32768 + (g >> 1) * 8192 + brow * 128 + (r & 3) * 8 + (((2 * (g & 1) + j) ^ bs) * 32);
      ad.b[1][j] = ad.b[0][j] ^ 32768;
    }
    asm volatile("" : "+v"(ad.a[0][0]), "+v"(ad.a[0][1]), "+v"(ad.a[1][0]), "+v"(ad.a[1][1]), "+v"(ad.b[0][0]), "+v"(ad.b[0][1]), "+v"(ad.b[1][0]), "+v"(ad.b[1][1]));
  }
  f32x4 acc[4][2];
#pragma unroll
  for (int t = 0; t < 4; ++t) {
    acc[t][0] = acc[t][1] = f32x4{0.f, 0.f, 0.f, 0.f};
    asm volatile("" : "+v"(acc[t][0]), "+v"(acc[t][1]));
  }
  {
    const u32 xa = K1_A + w * 10240 + lane * 16;
    float sa = 0.f, sb = 0.f, qa = 0.f, qb = 0.f;
#pragma unroll
    for (int j = 0; j < 10; ++j) {
      asm volatile("s_waitcnt vmcnt(%1)" : "+v"(c[j]) : "n"(9 - j));
      sa = dot2(c[j][0], 0x3f803f80u, sa); qa = dot2(c[j][0], c[j][0], qa);
      sb = dot2(c[j][1], 0x3f803f80u, sb); qb = dot2(c[j][1], c[j][1], qb);
      sa = dot2(c[j][2], 0x3f803f80u, sa); qa = dot2(c[j][2], c[j][2], qa);
      sb = dot2(c[j][3], 0x3f803f80u, sb); qb = dot2(c[j][3], c[j][3], qb);
      SCHED();
    }
    const f32x2 cf = ln_coef(sum8(sa + sb), sum8(qa + qb), 1.0f / D);
    const float ri = cf[0], nb = cf[1];
#pragma unroll
    for (int j = 0; j < 10; ++j) {
#pragma unroll
      for (int i = 0; i < 4; ++i) c[j][i] = pkbf(fma2(unpk(c[j][i]), f32x2{ri, ri}, f32x2{nb, nb}));
      asm volatile("ds_write_b128 %0, %1 offset:%2" ::"v"(xa), "v"(c[j]), "i"(j * 1024) : "memory");
    }
  }
  WAITL();
  BAR();
  FENCE();
  u32x2 v[3][2];
  if (grp == 1) {
    const int r0 = r >> 2, tg = 4 * (g >> 1) + q;
#pragma unroll
    for (int i = 0; i < 3; ++i) {
      const int kc = min(ct + 7 * i, 19);
      const int ch = 4 * (kc & 1) + 2 * (g & 1) + ((r & 3) >> 1);
      const u32 base = K1_A + (tg * 10 + (kc >> 1)) * 1024 + (r & 1) * 8;
      DSRTR(v[i][0], base + r0 * 128 + ((ch ^ r0) * 16), 0);
      DSRTR(v[i][1], base + (r0 + 4) * 128 + ((ch ^ (r0 + 4)) * 16), 0);
    }
#pragma unroll 1
    for (int i = 0; i < 10; ++i) BAR();
    asm volatile("s_waitcnt lgkmcnt(0)" : "+v"(v[0][0]), "+v"(v[0][1]), "+v"(v[1][0]), "+v"(v[1][1]), "+v"(v[2][0]), "+v"(v[2][1]));
#pragma unroll
    for (int i = 0; i < 3; ++i)
      if (ct + 7 * i < 20) gst128(ws + OFF_UT + (size_t)(32 * (ct + 7 * i) + 16 * (g & 1)) * 4096 + t0 * 2, r * 4096 + tg * 16, cat(v[i][0], v[i][1]));
  }
  k1_loop(ad, grp, acc);
  if (grp == 1) {
    arrive(C_N0, 4, ws + OFF_F0 + (rp * 8 + ct) * 128);
    BAR();
    const int nf = u1 * 4 + 2 * g;
    if (nf < 54) {
#pragma unroll
      for (int t = 0; t < 4; ++t)
        gst128(ws + OFF_Z1A + (size_t)((rp * 4 + t) * 27 + (nf >> 1)) * 1024, lane * 16,
               u32x4{pkbf(gelu2(f32x2{acc[t][0][0], acc[t][0][1]})), pkbf(gelu2(f32x2{acc[t][0][2], acc[t][0][3]})),
                     pkbf(gelu2(f32x2{acc[t][1][0], acc[t][1][1]})), pkbf(gelu2(f32x2{acc[t][1][2], acc[t][1][3]}))});
    }
  } else {
    const int ff = u2 * 4 + 2 * g - 54;
    const u32x2 ep = lds<u32x2>(C_TAG);
    u32x4 z[4];
    const u32x4 ones = {0x3f803f80u, 0x3f803f80u, 0x3f803f80u, 0x3f803f80u};
#pragma unroll
    for (int t = 0; t < 4; ++t)
#pragma unroll
      for (int i = 0; i < 4; ++i) z[t][i] = pkbf(gelu2(f32x2{acc[t][i >> 1][2 * (i & 1)], acc[t][i >> 1][2 * (i & 1) + 1]}));
#pragma unroll
    for (int t = 0; t < 4; ++t) {
      acc[t][0] = mma(ones, z[t], f32x4{0.f, 0.f, 0.f, 0.f});
      acc[t][1] = mma(z[t], z[t], f32x4{0.f, 0.f, 0.f, 0.f});
    }
    asm volatile("" : "+v"(z[0]), "+v"(z[1]), "+v"(z[2]), "+v"(z[3]));
    SCHED();
    BAR();
    SCHED();
    if (q == (r >> 2)) {
#pragma unroll
      for (int t = 0; t < 4; ++t)
        sts<f32x2>(K1_PART + (g * 64 + t * 16 + r) * 8, ff < 0 ? f32x2{0.f, 0.f} : f32x2{acc[t][0][0], diag4(acc[t][1], r)});
    }
    WAITL();
    BAR();
    FENCE();
    char* gr = ws + OFF_ST + (size_t)rp * 8192;
    if (g == 0) {
      f32x2 pp2[4];
#pragma unroll
      for (int i = 0; i < 4; ++i) pp2[i] = lds<f32x2>(K1_PART + (i * 64 + lane) * 8);
      const float s1 = opq(opq(pp2[0][0] + pp2[1][0]) + opq(pp2[2][0] + pp2[3][0]));
      const float s2 = opq(opq(pp2[0][1] + pp2[1][1]) + opq(pp2[2][1] + pp2[3][1]));
      const u32x4 gv = {__float_as_uint(s1), ep[0], __float_as_uint(s2), ep[1]};
      asm volatile("global_store_dwordx4 %0, %1, %2 sc1\n\ts_nop 1" ::"v"(ct * 1024 + lane * 16), "v"(gv), "s"(gr) : "memory");
    }
    BAR();
    {
      const u32 tb = K1_KG + g * 2048;
      const u32 wa = tb + ((r & 3) | ((r >> 3) << 2) | (((r >> 2) & 1) << 3)) * 32 + q * 8;
      const u32 ra = tb + (16 * (q >> 1) + 4 * (q & 1) + (r >> 2)) * 32 + (r & 3) * 8;
      u32x2 v[4][2];
#pragma unroll
      for (int j = 0; j < 2; ++j) {
#pragma unroll
        for (int t = 0; t < 4; ++t) asm volatile("ds_write_b64 %0, %1 offset:%2" ::"v"(wa), "v"(u32x2{z[t][2 * j], z[t][2 * j + 1]}), "i"(t * 512) : "memory");
#pragma unroll
        for (int i = 0; i < 2; ++i) { DSRTR(v[2 * j + i][0], ra, i * 1024); DSRTR(v[2 * j + i][1], ra, i * 1024 + 256); }
        BAR();
      }
      asm volatile("s_waitcnt lgkmcnt(0)" : "+v"(v[0][0]), "+v"(v[0][1]), "+v"(v[1][0]), "+v"(v[1][1]), "+v"(v[2][0]), "+v"(v[2][1]), "+v"(v[3][0]), "+v"(v[3][1]));
#pragma unroll
      for (int i = 0; i < 4; ++i) z[i] = cat(v[i][0], v[i][1]);
    }
    u32x4 p[7];
    float s1, s2;
    auto poll_issue = [&]() {
      const u32 vo = lane * 16;
#pragma unroll
      for (int i = 0; i < 7; ++i)
        asm volatile("global_load_dwordx4 %0, %1, %2 offset:%3 sc1" : "=v"(p[i]) : "v"(vo), "s"(gr + (i >> 2) * 4096), "i"((i & 3) * 1024) : "memory");
    };
    auto poll_check = [&]() {
      asm volatile("s_waitcnt vmcnt(0)" : "+v"(p[0]), "+v"(p[1]), "+v"(p[2]), "+v"(p[3]), "+v"(p[4]), "+v"(p[5]), "+v"(p[6])::"memory");
      u32 bad = 0;
      s1 = __uint_as_float(p[0][0]); s2 = __uint_as_float(p[0][2]);
#pragma unroll
      for (int i = 0; i < 7; ++i) {
        bad = __builtin_amdgcn_bitop3_b32(bad, p[i][1], ep[0], 0xF6);
        bad = __builtin_amdgcn_bitop3_b32(bad, p[i][3], ep[1], 0xF6);
        if (i > 0) { s1 = opq(s1 + __uint_as_float(p[i][0])); s2 = opq(s2 + __uint_as_float(p[i][2])); }
      }
      return __all(bad == 0) != 0;
    };
    float zf[4][8];
    auto widen = [&](int i) {
#pragma unroll
      for (int e = 0; e < 4; ++e) { zf[i][2 * e] = bflo(z[i][e]); zf[i][2 * e + 1] = bfhi(z[i][e]); PINV(zf[i][2 * e]); PINV(zf[i][2 * e + 1]); }
    };
    widen(0); widen(1);
    BAR();
    if (g == 0) poll_issue();
    widen(2); widen(3);
    BAR();
    if (g == 0) {
      BAR();
      bool done = poll_check();
      for (int guard = 0; !done && guard < (1 << 16); ++guard) {
        poll_issue();
        done = poll_check();
      }
      const f32x2 cf = ln_coef(s1, s2, 1.0f / FH);
      sts<float>(K1_STAT + lane * 4, cf[0]);
      sts<float>(K1_STAT + 256 + lane * 4, cf[1]);
      DSW32(K1_RDY, 1, 0);
    } else {
      BAR();
      lds_wait(K1_RDY);
    }
    if (ff >= 0) {
      f32x4 ri[2][2], nb[2][2];
#pragma unroll
      for (int sc = 0; sc < 2; ++sc)
#pragma unroll
        for (int h = 0; h < 2; ++h) {
          ri[sc][h] = lds<f32x4>(K1_STAT + (sc * 32 + q * 8 + h * 4) * 4);
          nb[sc][h] = lds<f32x4>(K1_STAT + 256 + (sc * 32 + q * 8 + h * 4) * 4);
        }
#pragma unroll
      for (int sc = 0; sc < 2; ++sc)
#pragma unroll
        for (int j = 0; j < 2; ++j) {
          u32x4 o;
#pragma unroll
          for (int i = 0; i < 4; ++i)
            o[i] = pkbf(opq(__builtin_fmaf(zf[2 * j + sc][2 * i], ri[sc][i >> 1][2 * (i & 1)], nb[sc][i >> 1][2 * (i & 1)])),
                        opq(__builtin_fmaf(zf[2 * j + sc][2 * i + 1], ri[sc][i >> 1][2 * (i & 1) + 1], nb[sc][i >> 1][2 * (i & 1) + 1])));
          gst128(ws + OFF_Z2F + (size_t)(ff + j) * ZS + (rp * 2 + sc) * 1024, lane * 16, o);
        }
    }
  }
}

DEVI void k1_aux(int aid, const KgPar& kp, const u16* wsp, char* ws) {
  const int lane = lane_id();
  const int w = __builtin_amdgcn_readfirstlane(threadIdx.x >> 6);
  const int r = lane & 15, q = lane >> 4;
  const u32 sm = w * 12800;
  KgReg k0;
  const int dbase = w < 4 ? 384 + aid * 8 : aid * 12 + (w < 8 ? 0 : 8), d0 = dbase + (w & 3), d1 = dbase + (w < 8 ? 8 : 4);
  kg_load(k0, kp, d0, lane);
  if (w == 9) {
    u32x2 ep;
    read_epoch(ep, ws);
    if (lane < 14) sts<u32>(C_N1 + lane * 4, 0);
    asm volatile("s_waitcnt vmcnt(0)" : "+v"(ep)::"memory");
    sts<u32x2>(C_TAG, ep + u32x2{1u, 1u});
    WAITL();
  }
  BAR();
  if (w >= 8 && (w - 8) * 8 < GL) glds4(wsp, (u32)(blockIdx.x * 8 + (lane >> 3)) * 4096u + ((w - 8) * 8 + (lane & 7)) * 128u, 12 * 12800 + 4096);
  static_assert(GL == 24, "one request of 8 rows x 8 lines");
  auto rest = [&](int i) { glds4(wsp, (u32)(aid * 64 + (w * 2 + i) * 8 + (lane >> 3)) * 4096u + (GL + (lane & 7)) * 128u, 12 * 12800 + 4096); };
  kgen(kp, k0, d0, d1, sm, lane, ws, [&](int d) { if (w < 4 && d == d0) rest(0); });
  if (w < 4) {
    arrive(C_N0, 4, ws + OFF_F0 + (aid * 8 + 7) * 128);
    rest(1);
  }
}

constexpr int K2_NW = 6, K2_NZ = 7, K2_Z = K2_NW * 12288;
constexpr int CV_SLOT = 17664, CV_R1 = 4224, CV_U2 = 8832, CV_U0 = 13472;
constexpr int k3_unit(int u) { return (u & 1) ? ((26 + u / 2) % K2_NW) * 12288 : K2_Z + ((25 + u / 2) % K2_NZ) * 12288; }
constexpr int K3_Y = k3_unit(12), K2_XB = k3_unit(11) + 3072, K2_XF = C_XF;
constexpr int K2_LDS = (K2_NW + K2_NZ) * 12288;
static_assert(8 * CV_SLOT + 256 <= K2_LDS, "lds");

struct K2Fr { u32x4 w[2][3], z[2][3]; };
#define K2_RW(N, KC, A) DSR128(N.w[KC][A], (KC) ? wO : wE, (A) * 2048)
#define K2_RZ(N, KC, C) DSR128(N.z[KC][C], zA, (C) * 2048 + (KC) * 1024)
#define K2_M(C, KC, A, CC) MMA(acc[A][CC], C.z[KC][CC], C.w[KC][A])
#define K2_STEP(C, N)                                                     \
  WAITL(); BAR(); SCHED();                                                \
  K2_M(C, 0, 0, 0); K2_RZ(N, 0, 0);                                       \
  K2_M(C, 0, 0, 1); K2_RW(N, 0, 0);                                       \
  K2_M(C, 0, 0, 2); K2_RZ(N, 0, 1);                                       \
  K2_M(C, 0, 1, 0); K2_RW(N, 0, 1);                                       \
  K2_M(C, 0, 1, 1); K2_RZ(N, 0, 2);                                       \
  K2_M(C, 0, 1, 2); K2_RW(N, 0, 2);                                       \
  K2_M(C, 0, 2, 0); K2_RZ(N, 1, 0);                                       \
  K2_M(C, 0, 2, 1); K2_RW(N, 1, 0);                                       \
  K2_M(C, 0, 2, 2); K2_RZ(N, 1, 1);                                       \
  K2_M(C, 1, 0, 0); K2_RW(N, 1, 1);                                       \
  K2_M(C, 1, 0, 1); K2_RZ(N, 1, 2);                                       \
  K2_M(C, 1, 0, 2); K2_RW(N, 1, 2); SCHED();                              \
  K2_M(C, 1, 1, 0); SCHED(); wE += dw; SCHED();                           \
  K2_M(C, 1, 1, 1); SCHED(); wO += dw; SCHED();                           \
  K2_M(C, 1, 1, 2); SCHED(); zA += dz; SCHED();                           \
  K2_M(C, 1, 2, 0); SCHED(); sw = sw == K2_NW - 1 ? 0 : sw + 1; dw = sw == 0 ? (u32)(-(K2_NW - 1) * 12288) : 12288u; SCHED(); \
  K2_M(C, 1, 2, 1); SCHED(); sz = sz == K2_NZ - 1 ? 0 : sz + 1; dz = sz == 0 ? (u32)(-(K2_NZ - 1) * 12288) : 12288u; SCHED(); \
  K2_M(C, 1, 2, 2); SCHED();

template <int U> DEVI void k3_half(const char* wo, int half, u32 vo) {
  const char* p = wo + U * 12288 + half * 6144;
  SETM0(k3_unit(U) + half * 6144);
  GLDS16C(p, vo, 0); GLDS16C(p, vo, 1024); GLDS16C(p, vo, 2048);
  SETM0(k3_unit(U) + half * 6144 + 3072);
  GLDS16C(p + 3072, vo, 0); GLDS16C(p + 3072, vo, 1024); GLDS16C(p + 3072, vo, 2048);
}
template <int NS, int U0, class Issue, class Hook>
DEVI void k2_loader(Issue issue, Hook hook, const char* wo, int half, u32 vo) {
  constexpr int E = U0 ? WE : 0, P = E > P0 ? E : P0;
#pragma unroll
  for (int s = 0; s < E; ++s) { issue(s); if (s == 1) hook(-1); }
  BAR();
#pragma unroll
  for (int s = E; s < P; ++s) issue(s);
  WAITV(6 * (P - 1));
  BAR();
#pragma unroll
  for (int s = P; s < NS; ++s) issue(s);
#pragma unroll 1
  for (int k = 0; k < 32 - NS; ++k) {
    WAITV(6 * (NS - 2));
    hook(k);
    BAR();
    issue(k + NS);
  }
  auto tail = [&](auto I) {
    constexpr int i = decltype(I)::value;
    if (i < NS - 1) {
      WAITV(6 * (NS - 2 - i + (i < WOE ? i : WOE)));
      BAR();
      if (i < WOE) k3_half<U0 + 2 * i>(wo, half, vo);
    }
  };
  tail(IC<0>{}); tail(IC<1>{}); tail(IC<2>{}); tail(IC<3>{}); tail(IC<4>{}); tail(IC<5>{});
}

DEVI void k2_gemm(int rt, int ct, const u16* wsp, const u16* bsp, char* ws, char* flag1) {
  const int lane = lane_id();
  const int w = wave2();
  const int r = lane & 15, q = lane >> 4;
  if (w >= 4) {
    const int lw = w - 4, prow = lane >> 3;
    const char* wo = ws + OFF_WOF + (size_t)((blockIdx.x >> 3) & 7) * 138240;
    u32 vo[6];
    if (lw < 2) {
#pragma unroll
      for (int i = 0; i < 6; ++i) vo[i] = (i >> 1) * ZS + (i & 1) * 1024 + lane * 16 - (i % 3) * 1024;
      const char* zb0 = ws + OFF_Z2F + (size_t)(ct * 6 + lw * 3) * ZS;
      u32 d = K2_Z + lw * 6144;
      const u32 fo2 = lane < 27 ? wo_flag(lane) : (min((rt * 96 + ((lane >> 1) & 3) * 32) >> 6, 31) * 8 + min((ct * 6 + (lane & 1) * 5) >> 3, 6)) * 128;
      unsigned long long seen = 0;
      k2_loader<K2_NZ, 0>([&](int s) {
        const char* zb = zb0 + s * 2048;
        SETM0(d); GLDS16C(zb, vo[0], 0); GLDS16C(zb, vo[1], 1024); GLDS16C(zb, vo[2], 2048);
        SETM0(d + 3072); GLDS16C(zb, vo[3], 0); GLDS16C(zb, vo[4], 1024); GLDS16C(zb, vo[5], 2048);
        d = d >= K2_Z + (K2_NZ - 1) * 12288 ? d - (K2_NZ - 1) * 12288 : d + 12288;
      }, [&](int k) {
        if (lw == 1 && k == 14) asm volatile("global_load_dwordx2 %0, %1, %2 sc1" : "+v"(seen) : "v"(fo2), "s"(ws + OFF_F2) : "memory");
        if (lw == 1 && k == 21) {
          asm volatile("" : "+v"(seen));
          if (!__all(seen == lds<unsigned long long>(C_TAG))) wait_flags(ws + OFF_F2, fo2);
        }
      }, wo, lw, lane * 16);
    } else {
#pragma unroll
      for (int i = 0; i < 6; ++i)
        vo[i] = (u32)(min(rt * 96 + ((lw - 2) * 6 + i) * 8, 2040) + prow) * 4096u + (((lane & 7) ^ prow) * 16) - (i % 3) * 1024;
      u32 d = (lw - 2) * 6144;
      k2_loader<K2_NW, 1>([&](int s) {
        const char* wb = (const char*)wsp + s * 128;
        SETM0(d); GLDS16(wb, vo[0], 0); GLDS16(wb, vo[1], 1024); GLDS16(wb, vo[2], 2048);
        SETM0(d + 3072); GLDS16(wb, vo[3], 0); GLDS16(wb, vo[4], 1024); GLDS16(wb, vo[5], 2048);
        d = d >= (K2_NW - 1) * 12288 ? d - (K2_NW - 1) * 12288 : d + 12288;
      }, [&](int k) { if (k < 0) arrive<12>(C_N1, 4, flag1); }, wo, lw - 2, lane * 16);
    }
    BAR();
    return;
  }
  const int tg = w >> 1, fg = w & 1;
  u32x2 z1v[3][3];
  f32x4 acc[3][3];
#pragma unroll
  for (int a = 0; a < 3; ++a) {
    const float bb = bf1(bsp[min(rt * 6 + tg * 3 + a, 127) * 16 + r]);
#pragma unroll
    for (int c = 0; c < 3; ++c) { acc[a][c] = f32x4{bb, bb, bb, bb}; asm volatile("" : "+v"(acc[a][c])); }
  }
  if (w == 1) {
    const int un = (54 + ct * 6 + (lane >> 5) * 5) >> 2;
    __builtin_amdgcn_s_sleep(GSL);
    wait_flags(ws + OFF_F1, ((lane & 31) * 8 + (un < 15 ? 6 : (un - 15) >> 1)) * 128);
  }
  BAR();
  u32 wE = (tg * 6 + (r >> 3)) * 1024 + (r & 7) * 128 + ((q ^ (r & 7)) * 16), wO = wE ^ 64;
  u32 zA = K2_Z + fg * 6144 + lane * 16;
  int sw = 2, sz = 2;
  u32 dw = 12288, dz = 12288;
  K2Fr fa, fb;
  WAITL();
  BAR();
  SCHED();
#pragma unroll
  for (int kc = 0; kc < 2; ++kc)
#pragma unroll
    for (int i = 0; i < 3; ++i) { K2_RZ(fa, kc, i); K2_RW(fa, kc, i); }
  wE += 12288; wO += 12288; zA += 12288;
#pragma unroll 1
  for (int it = 0; it < 11; ++it) {
    K2_STEP(fa, fb)
    K2_STEP(fb, fa)
  }
#pragma unroll
  for (int a = 0; a < 3; ++a)
#pragma unroll
    for (int c = 0; c < 3; ++c)
      asm volatile("global_load_dwordx2 %0, %1, %2 sc1" : "=v"(z1v[a][c]) : "v"(lane * 16),
                   "s"(ws + OFF_Z1A + (size_t)(min(rt * 6 + tg * 3 + a, 127) * 27 + ct * 3 + ((fg * 3 + c) >> 1)) * 1024 + ((fg + c) & 1) * 8));
#pragma unroll 1
  for (int it = 11; it < 15; ++it) {
    K2_STEP(fa, fb)
    K2_STEP(fb, fa)
  }
  K2_STEP(fa, fb)
  WAITL();
  BAR();
  SCHED();
#pragma unroll
  for (int kc = 0; kc < 2; ++kc)
#pragma unroll
    for (int a = 0; a < 3; ++a)
#pragma unroll
      for (int c = 0; c < 3; ++c) K2_M(fb, kc, a, c);
  SCHED();
  asm volatile("s_waitcnt vmcnt(0)" : "+v"(z1v[0][0]), "+v"(z1v[0][1]), "+v"(z1v[0][2]), "+v"(z1v[1][0]), "+v"(z1v[1][1]), "+v"(z1v[1][2]),
               "+v"(z1v[2][0]), "+v"(z1v[2][1]), "+v"(z1v[2][2]));
  MMA_DRAIN();
  const u32 xa = K2_XB + tg * 3072 + lane * 16;
  char* hb = ws + OFF_HF + (size_t)((rt * 6 + tg * 3) * 27 + ct * 3) * 1024;
  const int nt = 128 - (rt * 6 + tg * 3);
  auto gate = [&](int a, int c) {
    return u32x2{pkbf(unpk(z1v[a][c][0]) * f32x2{acc[a][c][0], acc[a][c][1]}), pkbf(unpk(z1v[a][c][1]) * f32x2{acc[a][c][2], acc[a][c][3]})};
  };
  if (fg == 0) {
#pragma unroll
    for (int a = 0; a < 3; ++a) sts<u32x2>(xa + a * 1024, gate(a, 2));
    DSW32(K2_XF + w * 4, 1, 0);
#pragma unroll
    for (int a = 0; a < 3; ++a)
      if (a < nt) gst128(hb + a * 27648, lane * 16, cat(gate(a, 0), gate(a, 1)));
  } else {
#pragma unroll
    for (int a = 0; a < 3; ++a) sts<u32x2>(xa + a * 1024 + 8, gate(a, 0));
    DSW32(K2_XF + w * 4, 1, 0);
#pragma unroll
    for (int a = 0; a < 3; ++a)
      if (a < nt) gst128(hb + a * 27648 + 2048, lane * 16, cat(gate(a, 1), gate(a, 2)));
  }
  lds_wait(K2_XF + (w ^ 1) * 4);
  if (fg < nt) gst128(hb + fg * 27648 + 1024, lane * 16, ld128(xa + fg * 1024));
  if (fg == 0 && 2 < nt) gst128(hb + 2 * 27648 + 1024, lane * 16, ld128(xa + 2048));
}

static_assert(4 * CV_SLOT <= K2_Z, "lds");
DEVI void k2_conv(int cid, char* ws) {
  char* flag = ws + OFF_CNT + (22 * 16 + cid) * FS;
  const int lane = lane_id();
  const int w = wave2();
  const u32 sm = w * CV_SLOT;
  const int r = lane & 15, q = lane >> 4;
  if (w < 4) __builtin_amdgcn_s_setprio(2);
  else __builtin_amdgcn_s_setprio(1);
  if (w == 1 || w == 2) {
    const int dd = min(cid * 12 + (lane >> 5) * 11, D - 1);
    if (w == 1 || cid >= 32) wait_flags(ws + OFF_F0, w == 1 ? ((lane & 31) * 8 + (dd >> 5) % 7) * 128 : ((min(cid * 12 + (lane & 15), D - 1) - 384) >> 3) * 1024 + 7 * 128);
  }
  BAR();
  const u32 vo = lane * 16;
  const u32 yo = (r & 1) * 512 + (32 * (r >> 1) + 8 * q) * 2;
#pragma unroll 1
  for (int c = w; c < 12; c += 8) {
    const int d = cid * 12 + c;
    if (d >= D) break;
    {
      const char* kr = ws + OFF_KR + (size_t)d * 4096;
      const char* ut = ws + OFF_UT + (size_t)d * 4096;
#define CV_LD4(P, V, DST) SETM0(DST); GLDS16C(P, V, 0); GLDS16C(P, V, 1024); GLDS16C(P, V, 2048); GLDS16C(P, V, 3072)
      CV_LD4(kr, vo, sm + CV_R1); CV_LD4(ut, vo, sm + CV_U0); CV_LD4(kr, vo + 2, sm); CV_LD4(ut, vo, sm + CV_U2 + 4);
    }
    if (c < 8) {
      if (lane < 32) sts<u32>(sm + 4096 + lane * 4, 0);
#pragma unroll
      for (int i = 0; i < 3; ++i) {
        const int o = lane + 64 * i;
        if (o < 129) sts<u32>(sm + 8320 + o * 4, 0);
        if (o < 135) sts<u32>(sm + 12932 + o * 4, 0);
      }
    }
    u32 fp = sm + (r & 1) * CV_R1 + 2 * (2044 - 4 * (r >> 1) + 8 * q) - 3584;
    u32 gp = sm + ((r & 1) ? CV_U0 : CV_U2) + 64 * (r >> 1) + 16 * q;
    f32x4 acc[8];
#pragma unroll
    for (int n = 0; n < 8; ++n) { acc[n] = f32x4{0, 0, 0, 0}; asm volatile("" : "+v"(acc[n])); }
    WAITV(0);
    if (lane == 0) sts<u16>(sm + 4094, 0);
    FENCE();
    struct Fr { u32x2 f[8][2]; u32x4 g[8]; };
    Fr fa, fb;
#define CV_RF1(N, I, H) DSR64(N.f[I][H], fp, (7 - (I)) * 512 + (H) * 8)
#define CV_RG(N, I) DSR128(N.g[I], gp, (I) * 512)
#define CV_M(C, NN, MU) MMA(acc[NN], cat(C.f[MU][0], C.f[MU][1]), C.g[(NN) - (MU)])
#define CV_MM(C, NN) _Pragma("unroll") for (int mu = 0; mu <= (NN); ++mu) { CV_M(C, NN, mu); }
#define CV_STEP(C, N)                                                                          \
    WAITL(); SCHED();                                                                          \
    _Pragma("unroll") for (int i = 0; i < 8; ++i) { CV_M(C, 7, i); CV_RG(N, i); }              \
    _Pragma("unroll") for (int i = 0; i < 7; ++i) { CV_M(C, 6, i); CV_RF1(N, i, 0); }          \
    _Pragma("unroll") for (int i = 0; i < 6; ++i) { CV_M(C, 5, i); CV_RF1(N, i, 1); }          \
    CV_M(C, 4, 0); CV_RF1(N, 7, 0); CV_M(C, 4, 1); CV_RF1(N, 6, 1); CV_M(C, 4, 2); CV_RF1(N, 7, 1); \
    CV_M(C, 4, 3); SCHED(); fp -= 64; SCHED(); CV_M(C, 4, 4); SCHED(); gp -= 64; SCHED();      \
    CV_MM(C, 3) CV_MM(C, 2) CV_MM(C, 1) CV_MM(C, 0)
#pragma unroll
    for (int i = 0; i < 8; ++i) { CV_RF1(fa, i, 0); CV_RF1(fa, i, 1); CV_RG(fa, i); }
    fp -= 64; gp -= 64;
#pragma unroll 1
    for (int it = 0; it < 3; ++it) {
      CV_STEP(fa, fb)
      CV_STEP(fb, fa)
    }
    CV_STEP(fa, fb)
    WAITL(); SCHED();
    CV_MM(fb, 0) CV_MM(fb, 1) CV_MM(fb, 2) CV_MM(fb, 3) CV_MM(fb, 4) CV_MM(fb, 5) CV_MM(fb, 6) CV_MM(fb, 7)
    SCHED();
    MMA_DRAIN();
#pragma unroll
    for (int n = 0; n < 8; n += 2) {
      const u32 a0 = pkbf(acc[n][0], acc[n][1]), a1 = pkbf(acc[n][2], acc[n][3]);
      const u32 b0 = pkbf(acc[n + 1][0], acc[n + 1][1]), b1 = pkbf(acc[n + 1][2], acc[n + 1][3]);
      const u32 g0 = dppu<0xB1>((r & 1) ? a0 : b0), g1 = dppu<0xB1>((r & 1) ? a1 : b1);
      gst128(ws + OFF_YT + (size_t)d * 4096 + n * 512, yo, (r & 1) ? u32x4{g0, b0, g1, b1} : u32x4{a0, g0, a1, g1});
    }
    __builtin_amdgcn_s_setprio(0);
  }
  if (w >= 4) {
    arrive(C_N2, 8, flag);
    if (w == 4) wait_flags(ws + OFF_F2, wo_flag(min(lane, 26)));
    if (lane == 0) __hip_atomic_fetch_add((LAS u32*)(uintptr_t)(C_N3), 1u, __ATOMIC_RELAXED, __HIP_MEMORY_SCOPE_WORKGROUP);
    for (int guard = 0; guard < (1 << 20); ++guard) {
      u32 v;
      asm volatile("ds_read_b32 %0, %1\n\ts_waitcnt lgkmcnt(0)" : "=v"(v) : "v"(C_N3) : "memory");
      if (rfl(v) >= 4) break;
      __builtin_amdgcn_s_sleep(2);
    }
    const char* wo = ws + OFF_WOF + (size_t)((blockIdx.x >> 3) & 7) * 138240;
    if (w < 6) { k3_half<0>(wo, w & 1, vo); k3_half<4>(wo, w & 1, vo); k3_half<8>(wo, w & 1, vo); }
    else { k3_half<2>(wo, w & 1, vo); k3_half<6>(wo, w & 1, vo); k3_half<10>(wo, w & 1, vo); }
  }
  BAR();
}

constexpr int k3_iss(int fc) { return K3W + K3S * fc < 27 ? K3W + K3S * fc : 27; }
DEVI void k3_phase(const u16* x, char* ws, u16* out, int flag, int nst) {
  const int b = blockIdx.x, xcd = b & 7, idx = b >> 3;
  const int rt = xcd * 4 + (idx >> 3), ct = idx & 7;
  const int lane = lane_id();
  const int w = wave2();
  const int r = lane & 15, q = lane >> 4;
  const u32 ba = lane * 16;
  if (w >= 4) {
    const int lw = w - 4;
    const char* wo = ws + OFF_WOF + (size_t)ct * 138240;
    BAR();
    auto last = [&]() { SETM0(k3_unit(11)); GLDS16C(wo + 11 * 12288, ba, 0); GLDS16C(wo + 11 * 12288, ba, 1024); GLDS16C(wo + 11 * 12288, ba, 2048); };
    if (nst == 8) {
      if (lw < 2) { k3_half<1>(wo, lw, ba); k3_half<5>(wo, lw, ba); k3_half<9>(wo, lw, ba); }
      else { k3_half<3>(wo, lw - 2, ba); k3_half<7>(wo, lw - 2, ba); if (lw == 2) last(); }
    } else {
      static_for<0, 6>([&](auto I) {
        constexpr int i = decltype(I)::value;
        if (i >= WOE || nst != 4) {
          if (lw < 2) k3_half<2 * i>(wo, lw, ba);
          else if (i < 5) k3_half<2 * i + 1>(wo, lw - 2, ba);
          else if (lw == 2) last();
        }
      });
    }
    if (lw == 2) {
      const u32 fo = (22 * 16 + min((ct * 80) / 12 + lane, (ct * 80 + 79) / 12)) * FS;
      const bool ok = look_flags(ws + OFF_CNT, fo);
      if (!ok) { BAR(); wait_flags(ws + OFF_CNT, fo); }
      u32x4 yv[10];
      const char* yb = ws + OFF_YT + (size_t)(ct * 80) * 4096 + rt * 128;
      const int row = lane >> 3;
#pragma unroll
      for (int i = 0; i < 10; ++i)
        asm volatile("global_load_dwordx4 %0, %1, %2 sc1" : "=v"(yv[i])
                     : "v"(row * 4096 + ((((lane & 7) >> 1) ^ ((row >> 1) & 1) ^ ((i & 1) << 1)) * 2 + (lane & 1)) * 16), "s"(yb + i * 32768));
      if (ok) BAR();
      asm volatile("s_waitcnt vmcnt(0)" : "+v"(yv[0]), "+v"(yv[1]), "+v"(yv[2]), "+v"(yv[3]), "+v"(yv[4]), "+v"(yv[5]), "+v"(yv[6]), "+v"(yv[7]), "+v"(yv[8]), "+v"(yv[9]));
#pragma unroll
      for (int i = 0; i < 10; ++i) sts<u32x4>(K3_Y + i * 1024 + ba, yv[i]);
      WAITL();
      BAR();
    } else {
      if (lw == 3) wait_flags(ws + OFF_CNT, (((rt * 64 + (lane < 9 ? 0 : 63)) / 96) * 16 + (lane < 9 ? lane : min(lane - 9, 8))) * FS);
      WAITV(0);
      BAR();
    }
    return;
  }
  BAR();
  if (nst) arrive(C_N2, nst, ws + OFF_CNT + flag);
  u32x4 hf[27], xv[2];
  u32x2 xs;
  const u32 xo = r * (D * 2) + q * 16, xo2 = r * (D * 2) + 128 + q * 8;
  {
    const char* xb = (const char*)x + (size_t)(rt * 64 + w * 16) * (D * 2) + ct * 160;
    GLD128(xv[0], xo, xb, 0); GLD128(xv[1], xo, xb, 64);
    asm volatile("global_load_dwordx2 %0, %1, %2" : "=v"(xs) : "v"(xo2), "s"(xb));
  }
  const char* hs = ws + OFF_HF + (size_t)((rt * 4 + w) * 27) * 1024;
  const u32 ba2 = ba + 65536, ba3 = ba + 131072;
  f32x4 acc[5];
#pragma unroll
  for (int i = 0; i < 5; ++i) { acc[i] = f32x4{0, 0, 0, 0}; asm volatile("" : "+v"(acc[i])); }
  u32x4 bf[3][5];
  u32x2 yv[5];
  f32x2 sres[10];
  u32 ya, yb;
  {
    const int r1 = 8 * q + (r >> 2), r2 = 64 + 4 * q + (r >> 2);
    ya = K3_Y + r1 * 128 + ((w ^ (((r1 >> 1) & 1) | (((r1 >> 3) & 1) << 1))) * 32) + (r & 3) * 8;
    yb = K3_Y + r2 * 128 + ((w ^ (((r2 >> 1) & 1) | (((r2 >> 3) & 1) << 1))) * 32) + (r & 3) * 8;
    asm volatile("" : "+v"(ya), "+v"(yb));
  }
  BAR();
  SCHED();
  auto ldh = [&](auto I) {
    constexpr int i = decltype(I)::value;
    u32x4 t;
    const u32 vo = ba;
    const char* p = hs + (i >> 2) * 4096;
    asm volatile("global_load_dwordx4 %0, %1, %2 offset:%3 sc1" : "=v"(t) : "v"(vo), "s"(p), "i"((i & 3) * 1024));
    hf[i] = t;
  };
  static_for<0, K3W>(ldh);
  auto rd = [&](auto FC, auto DF) {
    constexpr int fc = decltype(FC)::value, df = decltype(DF)::value;
    constexpr int ad = k3_unit((fc * 5 + df) / 12) + ((fc * 5 + df) % 12) * 1024;
    const u32 base = ad < 65536 ? ba : ad < 131072 ? ba2 : ba3;
    u32x4 t;
    DSR128(t, base, ad & 65535);
    bf[fc % 3][df] = t;
  };
  static_for<0, 5>([&](auto DF) { rd(IC<0>{}, DF); });
  static_for<0, 5>([&](auto DF) { rd(IC<1>{}, DF); });
  static_for<0, 27>([&](auto FC) {
    constexpr int fc = decltype(FC)::value;
    u32x4 hcur = hf[fc];
    asm volatile("s_waitcnt vmcnt(%1) lgkmcnt(%2)" : "+v"(hcur) : "n"(k3_iss(fc) - fc - 1), "n"(fc == 26 ? 0 : fc == K3YF + 1 ? 10 : 5));
    SCHED();
    static_for<0, 5>([&](auto DF) {
      constexpr int df = decltype(DF)::value;
      f32x4 c = acc[df];
      const u32x4 bb = bf[fc % 3][df], hh = hcur;
      MMA(c, bb, hh);
      acc[df] = c;
      if constexpr (fc + 2 < 27) rd(IC<fc + 2>{}, DF);
    });
    static_for<k3_iss(fc), k3_iss(fc + 1)>(ldh);
    if constexpr (fc == K3YF) {
      BAR();
      const u32 ya_ = ya, yb_ = yb;
      u32x2 t0, t1, t2, t3, t4;
      DSRTR(t0, ya_, 0); DSRTR(t1, ya_, 512); DSRTR(t2, ya_, 4096); DSRTR(t3, ya_, 4608); DSRTR(t4, yb_, 0);
      yv[0] = t0; yv[1] = t1; yv[2] = t2; yv[3] = t3; yv[4] = t4;
    }
    if constexpr (fc == K3YF + 2) {
      u32x2 y[5] = {yv[0], yv[1], yv[2], yv[3], yv[4]}, xs_ = xs;
      u32x4 x0 = xv[0], x1 = xv[1];
      asm volatile("" : "+v"(y[0]), "+v"(y[1]), "+v"(y[2]), "+v"(y[3]), "+v"(y[4]), "+v"(x0), "+v"(x1), "+v"(xs_));
#pragma unroll
      for (int i = 0; i < 10; ++i) {
        const u32 xx = i < 4 ? x0[i & 3] : i < 8 ? x1[i & 3] : xs_[i & 1], yy = y[i >> 1][i & 1];
        f32x2 t = {opq(bflo(xx) + bflo(yy)), opq(bfhi(xx) + bfhi(yy))};
        asm volatile("" : "+v"(t));
        sres[i] = t;
      }
    }
    SCHED();
  });
  MMA_DRAIN();
  u32 o[10];
#pragma unroll
  for (int i = 0; i < 10; ++i) o[i] = pkbf(sres[i] + f32x2{acc[i >> 1][2 * (i & 1)], acc[i >> 1][2 * (i & 1) + 1]});
  char* ob = (char*)out + (size_t)(rt * 64 + w * 16) * (D * 2) + ct * 160;
  *(GAS u32x4*)(ob + xo) = u32x4{o[0], o[1], o[2], o[3]};
  *(GAS u32x4*)(ob + xo + 64) = u32x4{o[4], o[5], o[6], o[7]};
  *(GAS u32x2*)(ob + xo2) = u32x2{o[8], o[9]};
}

extern "C" __global__ __launch_bounds__(768, 3) void k0(const u16* x, const u16* lar, const u16* aim, const u16* cre, const u16* cim,
                                                        const u16* ldt, const u16* dsk, const u16* win, const u16* wsp,
                                                        const u16* bsp, const u16* wout, char* ws, u16* out) {
  const int b = blockIdx.x, xcd = b & 7, idx = b >> 3;
  const int wp = __builtin_amdgcn_readfirstlane(threadIdx.x >> 6);
  if (idx == 0 && wp >= 1 && wp < 4) {
    unsigned long long base;
    u32 len;
    asm volatile("s_getpc_b64 %0\nk0_pc:\n\ts_mov_b32 %1, kz-k0_pc" : "=s"(base), "=s"(len));
    const u32 t = threadIdx.x - 64;
    glds4((const void*)base, min(t * 128u, len & ~3u), K1_SCR);
    glds4((const void*)base, min(t * 128u + 24576u, len & ~3u), K1_SCR);
  }
  int f1;
  if (idx < 28) {
    const int id = (xcd & 1) * 28 + idx, rp = (xcd >> 1) * 8 + (id & 7), ct = id >> 3;
    k1_gemm(rp, ct, idx, b, wout, x, win, wsp, ws);
    f1 = rp * 8 + ct;
  } else {
    const KgPar kp = {lar, aim, cre, cim, ldt, dsk};
    const int aid = xcd * 4 + (idx - 28);
    k1_aux(aid, kp, wsp, ws);
    f1 = aid * 8 + 7;
  }
  int r0, nr, c0, nc;
  k2_region(xcd, r0, nr, c0, nc);
  const bool isg = idx < nr * nc || (idx == 24 && (xcd & 3) == 1);
  char* flag1 = ws + OFF_F1 + f1 * 128;
  WAITL();
  if (idx >= 28) {
    arrive(C_N1, 12, flag1);
  } else if (wp >= 8) {
    BAR();
    arrive(C_N4, 5, ws + OFF_F2 + f1 * 128);
  } else {
    BAR();
    if (wp == 0) arrive(C_N4, 5, ws + OFF_F2 + f1 * 128);
    if (wp >= 4 && (wp < 6 || !isg)) arrive(C_N1, 4, flag1);
  }
  if (wp < 8) {
    int flag = 0, nst = 0;
    const int cid = idx >= 28 ? xcd * 4 + idx - 28 : 32 + xcd * 3 + idx - 25;
    if (isg) {
      int rt = r0 + 5, ct = 4;
      if (idx < nr * nc) { ct = idx / nr; rt = r0 + idx - ct * nr; ct += c0; }
      k2_gemm(rt, ct, wsp, bsp, ws, flag1);
      flag = (rt * 16 + ct) * FS; nst = 4;
    } else if (idx >= 25 && cid < 54) {
      k2_conv(cid, ws);
      flag = (22 * 16 + cid) * FS; nst = 8;
    } else {
      BAR();
      if (wp < 4) wait_flags(ws + OFF_F1, (wp * 64 + lane_id()) * 128);
      if (wp == 4) wait_flags(ws + OFF_F2, wo_flag(min(lane_id(), 26)));
      BAR();
      if (b == 27 * 8 + 7 && threadIdx.x == 0)
        __hip_atomic_store((GAS unsigned long long*)(ws + OFF_EP), lds<unsigned long long>(C_TAG), __ATOMIC_RELAXED, __HIP_MEMORY_SCOPE_AGENT);
    }
    k3_phase(x, ws, out, flag, nst);
  }
}

extern "C" __global__ void kz() {}

extern "C" void kernel_launch(void* const* d_in, const int* in_sizes, int n_in, void* d_out, int out_size, void* d_ws,
                              size_t ws_size, hipStream_t stream) {
  const u16* x = (const u16*)d_in[0];
  const u16* lar = (const u16*)d_in[1];
  const u16* aim = (const u16*)d_in[2];
  const u16* cre = (const u16*)d_in[3];
  const u16* cim = (const u16*)d_in[4];
  const u16* ldt = (const u16*)d_in[5];
  const u16* dsk = (const u16*)d_in[6];
  const u16* win = (const u16*)d_in[8];
  const u16* wsp = (const u16*)d_in[9];
  const u16* bsp = (const u16*)d_in[10];
  const u16* wout = (const u16*)d_in[11];
  char* ws = (char*)d_ws;
  (void)hipFuncSetAttribute((const void*)k0, hipFuncAttributeMaxDynamicSharedMemorySize, LDS_BYTES);
  k0<<<256, 768, LDS_BYTES, stream>>>(x, lar, aim, cre, cim, ldt, dsk, win, wsp, bsp, wout, ws, (u16*)d_out);
}
```
